```python
import torch

def _softmax(x, axis=-1):
    return torch.softmax(x, dim=axis)
def _gelu(x):
    return torch.nn.functional.gelu(x, approximate="tanh")
def _silu(x):
    return torch.nn.functional.silu(x)
def _sigmoid(x):
    return torch.sigmoid(x)
def _layernorm(x, axis=-1, eps=1e-5):
    m = x.mean(dim=axis, keepdim=True)
    v = x.var(dim=axis, keepdim=True, unbiased=False)
    return (x - m) / torch.sqrt(v + eps)
def _scatter_add(tgt, idx, src, axis=0):
    return tgt.clone().index_add_(axis, idx, src)
def _segment_sum(src, seg, n):
    out = torch.zeros((n,) + src.shape[1:], dtype=src.dtype)
    return out.index_add_(0, seg, src)
def _pad(x, pw):
    flat = tuple(v for pair in reversed(pw) for v in pair)
    return torch.nn.functional.pad(x, flat)

kernel_name = 'standalone__hybrid_graph_tf_graphgps__graph'

def setup_inputs():

    _w = (lambda a: (lambda t: t.to(torch.bfloat16) if t.is_floating_point() else t)(torch.as_tensor(a)))
    N, deg, D = 10000, 32, 128
    E = N * deg
    src = (lambda lo, hi, *s: torch.randint(lo, hi, s))(0, N, E).to(torch.int64)
    dst = (lambda lo, hi, *s: torch.randint(lo, hi, s))(0, N, E).to(torch.int64)
    deg_cnt = torch.bincount(dst, minlength=N).to(float) + 1.0
    sc = 1.0 / float(D) ** 0.5
    return {
        "x": _w((lambda *s: torch.randn(*s, dtype=torch.bfloat16) * 0.05)(N, D)),
        "src": _w(src),
        "dst": _w(dst),
        "deg_inv": _w((1.0 / deg_cnt).to(float)),
        "w_msg": _w((lambda *s: torch.randn(*s, dtype=torch.bfloat16) * 0.2)(D, D) * sc),
        "wq": _w((lambda *s: torch.randn(*s, dtype=torch.bfloat16) * 1.6)(D, D) * sc),
        "wk": _w((lambda *s: torch.randn(*s, dtype=torch.bfloat16) * 1.6)(D, D) * sc),
        "wv": _w((lambda *s: torch.randn(*s, dtype=torch.bfloat16) * 0.2)(D, D) * sc),
        "w_out": _w((lambda *s: torch.randn(*s, dtype=torch.bfloat16) * 0.2)(D, D) * sc),
    }

def reference(x, src, dst, deg_inv, w_msg, wq, wk, wv, w_out):
    N, D = x.shape[0], x.shape[1]
    h = _layernorm(x, axis=-1)
    m_e = torch.einsum('nd,de->ne', h, w_msg)
    h_local = _segment_sum(m_e[src], dst, N) * deg_inv[:, None]
    q = torch.einsum('nd,de->ne', h, wq)
    k = torch.einsum('nd,de->ne', h, wk)
    v = torch.einsum('nd,de->ne', h, wv)
    logits = torch.einsum('id,jd->ij', q, k) / (float(D) ** 0.5)
    attn = _softmax(logits, axis=-1)
    h_global = torch.einsum('ij,jd->id', attn, v)
    y = torch.einsum('nd,de->ne', h_local + h_global, w_out)
    return x + y
```

```cpp
#include <hip/hip_runtime.h>
#include <stdint.h>

typedef __bf16 bf16x8 __attribute__((ext_vector_type(8)));
typedef __bf16 bf16x4 __attribute__((ext_vector_type(4)));
typedef __bf16 bf16x2 __attribute__((ext_vector_type(2)));
typedef float f32x4 __attribute__((ext_vector_type(4)));
typedef float f32x2 __attribute__((ext_vector_type(2)));
typedef unsigned u32x4 __attribute__((ext_vector_type(4)));
typedef unsigned u32x2 __attribute__((ext_vector_type(2)));
typedef int i32x4 __attribute__((ext_vector_type(4)));
#define DI __device__ __forceinline__
#define SB() __builtin_amdgcn_sched_barrier(0)
#define LDSP(T) __attribute__((address_space(3))) T*

constexpr int N = 10000, D = 128, E = 320000;
constexpr int NG = 625;
constexpr int NQT = 5;
constexpr int NQB = 32, NSPLIT = 7, USTEP = 45;
constexpr int RING = 8;
static_assert(NQB * 4 * NQT == 640 && NSPLIT * USTEP * 32 >= N && NSPLIT * USTEP * 32 <= 10240, "geometry");
constexpr int ECH = 4096, NCH = (E + ECH - 1) / ECH;
constexpr int NTILE = 157;
constexpr int K1A = 160;
constexpr int NATT = NQB * NSPLIT, NSPARE = 256 - NATT;
static_assert(NATT % 8 == 0 && NSPARE % 8 == 0, "per-XCD roles");
static_assert(RING * 16384 + 3 * 4096 <= 4 * (12288 + 3 * 8192), "own fragments fit");
static_assert(4 * NQT <= 3 * NSPLIT && 4 * NQT > 3 * (NSPLIT - 1), "last stage: 3 groups per split");
constexpr int K3G = 3;
constexpr float BOUND_LIMIT = 100.f;
constexpr unsigned OFF_HROW = 1u << 20;
constexpr unsigned OFF_QF = 6u << 20;
constexpr unsigned OFF_AGG = 9u << 20;
constexpr unsigned OFF_ENT = 12u << 20;
constexpr unsigned OFF_CELL = 14u << 20;
constexpr unsigned OFF_WF = 17u << 20;
constexpr unsigned OFF_FLAG = (17u << 20) + (128u << 10);
constexpr unsigned OFF_CNT = (17u << 20) + (256u << 10);
constexpr unsigned OFF_PO = 20u << 20;
constexpr unsigned OFF_ST = 40u << 20;
constexpr unsigned WS_USED = 48u << 20;

DI unsigned xf(unsigned i) { const unsigned j = i >> 2; return (((j ^ (j >> 1)) & 1u) << 1) | (j >> 1); }
DI unsigned pk2(float lo, float hi) { f32x2 v = {lo, hi}; bf16x2 b = __builtin_convertvector(v, bf16x2); return __builtin_bit_cast(unsigned, b); }
DI float bflo(unsigned u) { return __uint_as_float(u << 16); }
DI float bfhi(unsigned u) { return __uint_as_float(u & 0xffff0000u); }
DI bf16x8 asbf8(u32x4 v) { return __builtin_bit_cast(bf16x8, v); }
DI f32x4 mfma16(bf16x8 a, bf16x8 b, f32x4 c) { return __builtin_amdgcn_mfma_f32_16x16x32_bf16(a, b, c, 0, 0, 0); }
DI bf16x4 trread(const char* p) { return __builtin_amdgcn_ds_read_tr16_b64_v4bf16((LDSP(bf16x4))p); }
DI bf16x8 cat8(bf16x4 a, bf16x4 b) { return __builtin_shufflevector(a, b, 0, 1, 2, 3, 4, 5, 6, 7); }
DI unsigned rfl(unsigned v) { return __builtin_amdgcn_readfirstlane(v); }
typedef __amdgpu_buffer_rsrc_t rsrc_t;
DI rsrc_t mk_rsrc(const void* p, unsigned bytes) { return __builtin_amdgcn_make_buffer_rsrc((void*)p, (short)0, (int)bytes, 0x00020000); }
DI u32x4 bld16(rsrc_t r, unsigned voff, unsigned soff) { return __builtin_bit_cast(u32x4, __builtin_amdgcn_raw_buffer_load_b128(r, (int)voff, (int)soff, 0)); }
template <int AUX> DI u32x4 bld16x(rsrc_t r, unsigned voff, unsigned soff) { return __builtin_bit_cast(u32x4, __builtin_amdgcn_raw_buffer_load_b128(r, (int)voff, (int)soff, AUX)); }
#define POAUX 0
DI unsigned bld4(rsrc_t r, unsigned voff, unsigned soff) { return __builtin_amdgcn_raw_buffer_load_b32(r, (int)voff, (int)soff, 0); }
DI void bst16(rsrc_t r, unsigned voff, unsigned soff, u32x4 v) { __builtin_amdgcn_raw_buffer_store_b128(__builtin_bit_cast(__attribute__((ext_vector_type(4))) int, v), r, (int)voff, (int)soff, 17); }
DI void bst16d(rsrc_t r, unsigned voff, unsigned soff, u32x4 v) { __builtin_amdgcn_raw_buffer_store_b128(__builtin_bit_cast(__attribute__((ext_vector_type(4))) int, v), r, (int)voff, (int)soff, 16); }
DI void bst8d(rsrc_t r, unsigned voff, unsigned soff, u32x2 v) { __builtin_amdgcn_raw_buffer_store_b64(__builtin_bit_cast(__attribute__((ext_vector_type(2))) int, v), r, (int)voff, (int)soff, 16); }
DI u32x4 bld16d(rsrc_t r, unsigned voff, unsigned soff) { return __builtin_bit_cast(u32x4, __builtin_amdgcn_raw_buffer_load_b128(r, (int)voff, (int)soff, 16)); }
typedef __attribute__((address_space(1))) unsigned gu32;
DI void wg_arrive(char* ws, unsigned idx) {
  asm volatile("s_waitcnt vmcnt(0)" ::: "memory");
  __syncthreads();
  if (threadIdx.x == 0) __hip_atomic_fetch_add((gu32*)(ws + OFF_CNT + idx * 256u), 1u, __ATOMIC_RELAXED, __HIP_MEMORY_SCOPE_AGENT);
}
DI void wg_arrive_stores(char* ws, unsigned idx) {
  if (threadIdx.x == 0) __hip_atomic_fetch_add((gu32*)(ws + OFF_CNT + idx * 256u), 1u, __ATOMIC_RELAXED, __HIP_MEMORY_SCOPE_AGENT);
}
template <bool DRAIN> DI void wg_wait2(char* ws, unsigned i0, unsigned want0, unsigned i1, unsigned want1) {
  if (threadIdx.x < 64u) {
    const bool second = threadIdx.x == 1u;
    gu32* p = (gu32*)(ws + OFF_CNT + (second ? i1 : i0) * 256u);
    const unsigned want = second ? want1 : want0;
    for (unsigned spins = 0; spins < (1u << 21); ++spins) {
      if (__all((int)(__hip_atomic_load(p, __ATOMIC_RELAXED, __HIP_MEMORY_SCOPE_AGENT) >= want))) break;
      __builtin_amdgcn_s_sleep(2);
    }
  }
  if (DRAIN) asm volatile("s_waitcnt vmcnt(0) lgkmcnt(0)" ::: "memory");
  asm volatile("s_barrier" ::: "memory");
}
constexpr unsigned OOB = 0x80000000u;
DI float xsum16_32(float v) {
  auto a = __builtin_amdgcn_permlane16_swap(__float_as_uint(v), __float_as_uint(v), false, false);
  v = __uint_as_float(a[0]) + __uint_as_float(a[1]);
  auto b = __builtin_amdgcn_permlane32_swap(__float_as_uint(v), __float_as_uint(v), false, false);
  return __uint_as_float(b[0]) + __uint_as_float(b[1]);
}
DI unsigned wscan_incl(unsigned v, unsigned) {
  v += __builtin_amdgcn_update_dpp(0u, v, 0x111, 0xf, 0xf, false);
  v += __builtin_amdgcn_update_dpp(0u, v, 0x112, 0xf, 0xf, false);
  v += __builtin_amdgcn_update_dpp(0u, v, 0x114, 0xf, 0xf, false);
  v += __builtin_amdgcn_update_dpp(0u, v, 0x118, 0xf, 0xf, false);
  v += __builtin_amdgcn_update_dpp(0u, v, 0x142, 0xa, 0xf, false);
  v += __builtin_amdgcn_update_dpp(0u, v, 0x143, 0xc, 0xf, false);
  return v;
}
DI unsigned code_touch(const char* base, unsigned lo, unsigned hi, unsigned idx) {
  const unsigned o = lo + idx * 128u;
  return *(const __attribute__((address_space(1))) unsigned*)(base + (o < hi ? o & ~3u : lo & ~3u));
}
#define CODE_PC(var, label) const char* var; asm volatile("s_getpc_b64 %0\n" label ":" : "=s"(var))
#define CODE_MARK(label) asm volatile(label ":")
#define CODE_OFF(var, a, b) unsigned var; asm volatile("s_mov_b32 %0, " a "-" b : "=s"(var))
constexpr float RT_D = 11.313708498984761f;
constexpr float HC = 1.0f / (11.313708498984761f - 1.0f);
constexpr float QSCALE = 0.12751743082f;

#define STAMP(ws, slot, idx) do { } while (0)
#define XCC(ws, slot) do { } while (0)
DI void dma_rows16(rsrc_t r, unsigned voff, unsigned sbase, unsigned m0) {
  asm volatile("s_mov_b32 m0, %0\n\ts_nop 0\n\t"
               "buffer_load_dwordx4 %1, %2, %3 offen lds\n\tbuffer_load_dwordx4 %1, %2, %4 offen offset:1024 lds\n\t"
               "buffer_load_dwordx4 %1, %2, %5 offen offset:2048 lds\n\tbuffer_load_dwordx4 %1, %2, %6 offen offset:3072 lds"
               :: "s"(m0), "v"(voff), "s"(r), "s"(sbase + 4096u), "s"(sbase + 3136u), "s"(sbase + 2176u), "s"(sbase + 1216u) : "memory");
}
DI void stage_matrix(const void* W, char* lds, unsigned kt0, int nkt, unsigned lane) {
  const rsrc_t r = mk_rsrc((const char*)W - 4096, 32768u + 4096u);
  const unsigned i = lane >> 2, voff = i * 256u + 16u * ((lane & 3u) ^ xf(i)), la = rfl((unsigned)(size_t)lds);
  for (int k = 0; k < nkt; ++k) dma_rows16(r, voff, rfl((kt0 + k) * 4096u), la + (kt0 + k) * 4096u);
}
#define VMWAIT0() asm volatile("s_waitcnt vmcnt(0)" ::: "memory")

__global__ __launch_bounds__(256, 1) void k1_prep(const uint16_t* __restrict__ x, const int* __restrict__ src, const int* __restrict__ dst,
                                               const uint16_t* __restrict__ wq, const uint16_t* __restrict__ wk, char* __restrict__ ws) {
  extern __shared__ __attribute__((aligned(16))) char lds[];
  const unsigned tid = threadIdx.x, lane = tid & 63u, w = rfl(tid >> 6), b = blockIdx.x;
  CODE_PC(pc1, "L_gps_k1");
  STAMP(ws, b * 4u + w, 0);
  XCC(ws, b * 4u + w);
  if (__builtin_expect(b >= (unsigned)(K1A + NCH), 0)) {
    CODE_OFF(lo, "L_gps_k2", "L_gps_k1"); CODE_OFF(h1, "L_gps_k2e", "L_gps_k1"); CODE_OFF(h2, "L_gps_k2f", "L_gps_k1");
    const unsigned hi = h1 > h2 ? h1 : h2;
    unsigned acc = 0;
#pragma unroll
    for (int i = 0; i < 2; ++i) acc ^= code_touch(pc1, lo, hi, i * 256u + tid);
    asm volatile("" :: "v"(acc));
    return;
  }
  const rsrc_t rw = mk_rsrc(ws, WS_USED);
  if (b == 0 && tid <= 2u * NQB) *(unsigned*)(ws + OFF_CNT + tid * 256u) = 0u;
  if (__builtin_expect(b < (unsigned)K1A, 1)) {
    const unsigned n = lane & 15u, g = lane >> 4;
    const unsigned node = 64u * b + 16u * w + n;
    const bool valid = node < (unsigned)N;
    const uint16_t* xr = x + (size_t)(valid ? node : 0u) * D + 4u * g;
    u32x2 xv[4][2];
#pragma unroll
    for (int ds = 0; ds < 4; ++ds)
#pragma unroll
      for (int hb = 0; hb < 2; ++hb) xv[ds][hb] = *(const u32x2*)(xr + 32 * ds + 16 * hb);
    CODE_OFF(c1e, "L_gps_k1e", "L_gps_k1");
    const unsigned ctouch = code_touch(pc1, 0u, c1e, tid & 127u);
    stage_matrix(w < 2 ? wq : wk, lds + (w >> 1) * 32768u, (w & 1u) * 4u, 4, lane);
    float z[4][2][4]; float s = 0.f;
#pragma unroll
    for (int ds = 0; ds < 4; ++ds)
#pragma unroll
      for (int hb = 0; hb < 2; ++hb) {
        z[ds][hb][0] = bflo(xv[ds][hb][0]); z[ds][hb][1] = bfhi(xv[ds][hb][0]); z[ds][hb][2] = bflo(xv[ds][hb][1]); z[ds][hb][3] = bfhi(xv[ds][hb][1]);
        s += (z[ds][hb][0] + z[ds][hb][1]) + (z[ds][hb][2] + z[ds][hb][3]);
      }
    STAMP(ws, b * 4u + w, 1);
    const float mean = xsum16_32(s) * (1.0f / D);
    float v2 = 0.f;
#pragma unroll
    for (int ds = 0; ds < 4; ++ds)
#pragma unroll
      for (int hb = 0; hb < 2; ++hb)
#pragma unroll
        for (int r = 0; r < 4; ++r) { const float d = z[ds][hb][r] - mean; z[ds][hb][r] = d; v2 += d * d; }
    const float rstd = valid ? __builtin_amdgcn_rsqf(xsum16_32(v2) * (1.0f / D) + 1e-5f) : 0.f;
#pragma unroll
    for (int ds = 0; ds < 4; ++ds)
#pragma unroll
      for (int hb = 0; hb < 2; ++hb)
#pragma unroll
        for (int r = 0; r < 4; ++r) z[ds][hb][r] *= rstd;
    const float cz = HC * xsum16_32(g == 3u ? z[3][1][3] : 0.f);
    bf16x8 zb[4]; u32x4 hq[4];
#pragma unroll
    for (int ds = 0; ds < 4; ++ds) {
      u32x4 a = {pk2(z[ds][0][0], z[ds][0][1]), pk2(z[ds][0][2], z[ds][0][3]), pk2(z[ds][1][0], z[ds][1][1]), pk2(z[ds][1][2], z[ds][1][3])};
      zb[ds] = asbf8(a);
      float last = z[ds][1][3] + cz;
      if (ds == 3 && g == 3) last = valid ? 1.0f : 0.f;
      hq[ds] = (u32x4){pk2(z[ds][0][0] + cz, z[ds][0][1] + cz), pk2(z[ds][0][2] + cz, z[ds][0][3] + cz), pk2(z[ds][1][0] + cz, z[ds][1][1] + cz), pk2(z[ds][1][2] + cz, last)};
    }
    STAMP(ws, b * 4u + w, 2);
    asm volatile("" :: "v"(ctouch));
    VMWAIT0();
    __syncthreads();
#pragma unroll
    for (int ds = 0; ds < 4; ++ds) bst16(rw, node * 256u + (4u * ds + g) * 16u, OFF_HROW, hq[ds]);
    STAMP(ws, b * 4u + w, 3);
    const unsigned q4 = n >> 2, p = lane & 3u, iv = 4u * g + q4;
    const unsigned ab = iv * 64u + 16u * (p ^ xf(iv));
    const unsigned kb = 32768u + n * 64u + 16u * (g ^ xf(n));
    bf16x8 a1[8][4], a2[8][4];
#pragma unroll
    for (int et = 0; et < 8; ++et)
#pragma unroll
      for (int cs = 0; cs < 4; ++cs) { const char* a = lds + (2 * cs) * 4096 + (et >> 1) * 1024 + 8 * (et & 1) + ab; a1[et][cs] = cat8(trread(a), trread(a + 4096)); }
    SB();
#pragma unroll
    for (int dt = 0; dt < 8; ++dt)
#pragma unroll
      for (int es = 0; es < 4; ++es) a2[dt][es] = asbf8(*(const LDSP(u32x4))(lds + dt * 4096 + es * 1024 + kb));
    SB();
    f32x4 q1[8];
#pragma unroll
    for (int et = 0; et < 8; ++et) q1[et] = (f32x4){0.f, 0.f, 0.f, 0.f};
#pragma unroll
    for (int cs = 0; cs < 4; ++cs)
#pragma unroll
      for (int et = 0; et < 8; ++et) q1[et] = mfma16(a1[et][cs], zb[cs], q1[et]);
    bf16x8 qb[4];
#pragma unroll
    for (int es = 0; es < 4; ++es) {
      u32x4 a = {pk2(q1[2 * es][0], q1[2 * es][1]), pk2(q1[2 * es][2], q1[2 * es][3]), pk2(q1[2 * es + 1][0], q1[2 * es + 1][1]), pk2(q1[2 * es + 1][2], q1[2 * es + 1][3])};
      qb[es] = asbf8(a);
    }
    STAMP(ws, b * 4u + w, 4);
    f32x4 qp[8]; float s1 = 0.f;
#pragma unroll
    for (int dt = 0; dt < 8; ++dt) qp[dt] = (f32x4){0.f, 0.f, 0.f, 0.f};
#pragma unroll
    for (int es = 0; es < 4; ++es)
#pragma unroll
      for (int dt = 0; dt < 8; ++dt) qp[dt] = mfma16(a2[dt][es], qb[es], qp[dt]);
#pragma unroll
    for (int dt = 0; dt < 8; ++dt) s1 += (qp[dt][0] + qp[dt][1]) + (qp[dt][2] + qp[dt][3]);
    STAMP(ws, b * 4u + w, 5);
    s1 = xsum16_32(s1);
    const float q127 = xsum16_32(g == 3u ? qp[7][3] : 0.f);
    const float gam = (s1 * (1.0f / RT_D) - q127) * HC;
    float n2 = 0.f;
#pragma unroll
    for (int dt = 0; dt < 8; ++dt)
#pragma unroll
      for (int r = 0; r < 4; ++r) {
        float v = (qp[dt][r] - gam) * QSCALE;
        if (dt == 7 && r == 3 && g == 3) v = 0.f;
        qp[dt][r] = v; n2 += v * v;
      }
    n2 = xsum16_32(n2);
    const unsigned G = 4u * b + w;
#pragma unroll
    for (int ds = 0; ds < 4; ++ds) {
      u32x4 a = {pk2(qp[2 * ds][0], qp[2 * ds][1]), pk2(qp[2 * ds][2], qp[2 * ds][3]), pk2(qp[2 * ds + 1][0], qp[2 * ds + 1][1]), pk2(qp[2 * ds + 1][2], qp[2 * ds + 1][3])};
      bst16(rw, G * 4096u + ds * 1024u + lane * 16u, OFF_QF, a);
    }
    const bool unsafe = !(sqrtf(n2) * 11.45f <= BOUND_LIMIT);
    const unsigned long long any = __ballot(unsafe);
    if (lane == 0) *(unsigned*)(ws + OFF_FLAG + G * 4u) = any ? 1u : 0u;
    STAMP(ws, b * 4u + w, 6);
  } else {
    const unsigned c = b - K1A;
    unsigned* hist = (unsigned*)lds;
    unsigned* base = hist + 640;
    unsigned* sorted = hist + 1280;
    hist[tid] = 0u; hist[256 + tid] = 0u; if (tid < 128) hist[512 + tid] = 0u;
    i32x4 dv[4], sv[4];
#pragma unroll
    for (int i = 0; i < 4; ++i) {
      const unsigned e0 = c * ECH + i * 1024u + tid * 4u, ec = e0 < (unsigned)E ? e0 : 0u;
      dv[i] = *(const i32x4*)(dst + ec); sv[i] = *(const i32x4*)(src + ec);
      if (e0 >= (unsigned)E) dv[i] = (i32x4){-1, -1, -1, -1};
    }
    __syncthreads();
    STAMP(ws, b * 4u + w, 1);
    unsigned rank[4][4];
#pragma unroll
    for (int i = 0; i < 4; ++i)
#pragma unroll
      for (int k = 0; k < 4; ++k) { rank[i][k] = 0u; if (dv[i][k] >= 0) rank[i][k] = atomicAdd(&hist[(unsigned)dv[i][k] >> 4], 1u); }
    STAMP(ws, b * 4u + w, 2);
    __syncthreads();
    if (w == 0) {
      unsigned h[10], t = 0;
#pragma unroll
      for (int k = 0; k < 10; ++k) { h[k] = hist[10 * lane + k]; t += h[k]; }
      unsigned ex = wscan_incl(t, lane) - t;
#pragma unroll
      for (int k = 0; k < 10; ++k) { base[10 * lane + k] = ex; ex += h[k]; }
    }
    __syncthreads();
#pragma unroll
    for (int i = 0; i < 4; ++i)
#pragma unroll
      for (int k = 0; k < 4; ++k) rank[i][k] = dv[i][k] >= 0 ? rank[i][k] + base[(unsigned)dv[i][k] >> 4] : 4096u + lane;
    unsigned cb[3], chn[3];
#pragma unroll
    for (int k = 0; k < 3; ++k) { const unsigned gq = k * 256u + tid < 640u ? k * 256u + tid : 0u; cb[k] = base[gq]; chn[k] = hist[gq]; }
    SB();
#pragma unroll
    for (int i = 0; i < 4; ++i)
#pragma unroll
      for (int k = 0; k < 4; ++k) sorted[rank[i][k]] = ((unsigned)sv[i][k] << 8) | ((unsigned)dv[i][k] & 255u);
    STAMP(ws, b * 4u + w, 4);
#pragma unroll
    for (int k = 0; k < 3; ++k) { const unsigned gq = k * 256u + tid; if (gq < (unsigned)NG) *(unsigned*)(ws + OFF_CELL + (gq * 80u + c) * 4u) = cb[k] | (chn[k] << 16); }
    __syncthreads();
#pragma unroll
    for (int i = 0; i < 4; ++i) bst16(rw, (c * ECH + i * 1024u + tid * 4u) * 4u, OFF_ENT, *(const u32x4*)(sorted + i * 1024 + tid * 4));
    STAMP(ws, b * 4u + w, 6);
  }
  CODE_MARK("L_gps_k1e");
}

DI void make_selectors(unsigned lane, bf16x8& sa, bf16x8& sb) {
  const unsigned i = lane & 15u, g = lane >> 4;
  u32x4 a, b;
#pragma unroll
  for (int q = 0; q < 4; ++q) {
    const unsigned k0 = 8u * g + 2u * q;
    a[q] = (k0 == i ? 0x3f80u : 0u) | (k0 + 1u == i ? 0x3f800000u : 0u);
    b[q] = (k0 == i + 16u ? 0x3f80u : 0u) | (k0 + 1u == i + 16u ? 0x3f800000u : 0u);
  }
  sa = asbf8(a); sb = asbf8(b);
}

DI void weight_prep(const uint16_t* __restrict__ wa, const uint16_t* __restrict__ wout, char* __restrict__ ws, char* lds, int m, unsigned lane) {
  stage_matrix(wout, lds, 0u, 8, lane);
  const rsrc_t rw = mk_rsrc(ws, WS_USED);
  const unsigned f = lane & 15u, g = lane >> 4, q4 = f >> 2, p = lane & 3u;
  bf16x8 a[8][4];
#pragma unroll
  for (int dt = 0; dt < 8; ++dt)
#pragma unroll
    for (int es = 0; es < 4; ++es) a[dt][es] = asbf8(*(const u32x4*)(wa + (16 * dt + f) * D + 32 * es + 8 * g));
  SB();
  VMWAIT0();
#pragma unroll 1
  for (unsigned ft = 0; ft < 8u; ++ft) {
    bf16x8 bw[4];
#pragma unroll
    for (int es = 0; es < 4; ++es) {
      const unsigned i0 = 8u * (g & 1u) + q4, i1 = i0 + 4u, kt = 2u * es + (g >> 1);
      const char* bp = lds + kt * 4096u + (ft >> 1) * 1024u;
      bw[es] = cat8(trread(bp + i0 * 64u + 16u * ((2u * (ft & 1u) + (p >> 1)) ^ xf(i0)) + 8u * (p & 1u)), trread(bp + i1 * 64u + 16u * ((2u * (ft & 1u) + (p >> 1)) ^ xf(i1)) + 8u * (p & 1u)));
    }
    f32x4 acc[8];
#pragma unroll
    for (int dt = 0; dt < 8; ++dt) acc[dt] = (f32x4){0.f, 0.f, 0.f, 0.f};
#pragma unroll
    for (int es = 0; es < 4; ++es)
#pragma unroll
      for (int dt = 0; dt < 8; ++dt) acc[dt] = mfma16(a[dt][es], bw[es], acc[dt]);
    float s = 0.f;
#pragma unroll
    for (int dt = 0; dt < 8; ++dt) s += (acc[dt][0] + acc[dt][1]) + (acc[dt][2] + acc[dt][3]);
    s = xsum16_32(s);
    const float w127 = xsum16_32(g == 3u ? acc[7][3] : 0.f);
    const float t = (s * (1.0f / RT_D) - w127) * HC;
#pragma unroll
    for (int dt = 0; dt < 8; ++dt) {
      float v[4];
#pragma unroll
      for (int r = 0; r < 4; ++r) v[r] = (dt == 7 && r == 3 && g == 3) ? 0.f : acc[dt][r] - t;
      const unsigned s4 = dt >> 1;
      unsigned gd, jh;
      if (m == 1) { gd = 2u * (dt & 1) + (g >> 1); jh = g & 1u; }
      else        { gd = 2u * (g & 1u) + (dt & 1); jh = g >> 1; }
      u32x2 o = {pk2(v[0], v[1]), pk2(v[2], v[3])};
      bst8d(rw, m * 32768u + (ft * 4u + s4) * 1024u + (16u * gd + f) * 16u + 8u * jh, OFF_WF, o);
    }
  }
}

constexpr unsigned GBLK = 8192;
constexpr unsigned LISTB = 12288;
constexpr unsigned SPW = LISTB + 3 * GBLK;
constexpr int TPW = (NG + NSPARE * 4 - 3) / (NSPARE * 4 - 2);
static_assert(4 * TPW == 4 * NQT && NSPARE == NQB, "a spare workgroup aggregates the groups of one query block");
static_assert(TPW <= 8 && (NG + TPW - 1) / TPW <= NSPARE * 4 - 2 && SPW >= 32768, "spare roles");
constexpr unsigned NOEDGE = 0x7fffffffu;
DI unsigned agg_voff(unsigned e, unsigned i, unsigned y) { return (e & ~0xffu) | (16u * (y ^ xf(i))); }
DI rsrc_t agg_rsrc(const char* ws) { return mk_rsrc(ws + OFF_HROW - 4096, 10240u * 256u + 4096u); }
DI void agg_dma(rsrc_t rh, unsigned v0, unsigned v1, unsigned m0base) {
  asm volatile("s_mov_b32 m0, %0\n\ts_nop 0\n\t"
               "buffer_load_dwordx4 %2, %4, %5 offen lds\n\tbuffer_load_dwordx4 %2, %4, %6 offen offset:1024 lds\n\t"
               "buffer_load_dwordx4 %2, %4, %7 offen offset:2048 lds\n\tbuffer_load_dwordx4 %2, %4, %8 offen offset:3072 lds\n\t"
               "s_mov_b32 m0, %1\n\ts_nop 0\n\t"
               "buffer_load_dwordx4 %3, %4, %5 offen lds\n\tbuffer_load_dwordx4 %3, %4, %6 offen offset:1024 lds\n\t"
               "buffer_load_dwordx4 %3, %4, %7 offen offset:2048 lds\n\tbuffer_load_dwordx4 %3, %4, %8 offen offset:3072 lds"
               :: "s"(m0base), "s"(m0base + 4096u), "v"(v0), "v"(v1), "s"(rh), "s"(4096u), "s"(3136u), "s"(2176u), "s"(1216u) : "memory");
}
DI u32x4 agg_onehot(u32x4 ea, u32x4 eb, unsigned n) {
  u32x4 oh;
  oh[0] = ((ea[0] & 0xffu) == n ? 0x3f80u : 0u) | ((ea[1] & 0xffu) == n ? 0x3f800000u : 0u);
  oh[1] = ((ea[2] & 0xffu) == n ? 0x3f80u : 0u) | ((ea[3] & 0xffu) == n ? 0x3f800000u : 0u);
  oh[2] = ((eb[0] & 0xffu) == n ? 0x3f80u : 0u) | ((eb[1] & 0xffu) == n ? 0x3f800000u : 0u);
  oh[3] = ((eb[2] & 0xffu) == n ? 0x3f80u : 0u) | ((eb[3] & 0xffu) == n ? 0x3f800000u : 0u);
  return oh;
}
DI void agg_store(char* __restrict__ ws, unsigned G, unsigned lane, float dinv, const f32x4 (&acc)[8]) {
#pragma unroll
  for (int s4 = 0; s4 < 4; ++s4) {
    u32x4 a = {pk2(acc[2 * s4][0] * dinv, acc[2 * s4][1] * dinv), pk2(acc[2 * s4][2] * dinv, acc[2 * s4][3] * dinv),
               pk2(acc[2 * s4 + 1][0] * dinv, acc[2 * s4 + 1][1] * dinv), pk2(acc[2 * s4 + 1][2] * dinv, acc[2 * s4 + 1][3] * dinv)};
    bst16d(mk_rsrc(ws, WS_USED), G * 4096u + s4 * 1024u + lane * 16u, OFF_AGG, a);
  }
}
DI void agg_slow(char* __restrict__ ws, const uint16_t* __restrict__ deg_inv, char* L, unsigned G, unsigned lane) {
  unsigned* list = (unsigned*)L;
  char* ring = L + LISTB;
  const unsigned ringa = rfl((unsigned)(size_t)ring);
  const rsrc_t re = mk_rsrc(ws + OFF_ENT, NCH * ECH * 4u), rh = agg_rsrc(ws);
  const unsigned* cell = (const unsigned*)(ws + OFF_CELL) + G * 80u;
  const float dinv = __uint_as_float((unsigned)deg_inv[16u * G + (lane & 15u)] << 16);
  const unsigned n = lane & 15u, g = lane >> 4, q4 = n >> 2, p = lane & 3u, iv = 4u * g + q4, il = lane >> 2;
  const unsigned vb0 = iv * 64u + 16u * ((p >> 1) ^ xf(iv)) + 8u * (p & 1u), vb1 = vb0 ^ 32u, key = ((G & 15u) << 4) | n;
  f32x4 acc[8];
#pragma unroll
  for (int q = 0; q < 8; ++q) acc[q] = (f32x4){0.f, 0.f, 0.f, 0.f};
#pragma unroll 1
  for (int c = 0; c < NCH; ++c) {
    const unsigned cc = rfl(cell[c]), cs = cc & 0xffffu, cn = cc >> 16;
#pragma unroll 1
    for (unsigned k0 = 0; k0 < cn; k0 += 32u) {
      const unsigned k = k0 + (lane & 31u);
      const unsigned ev = bld4(re, k < cn ? (c * ECH + cs + k) * 4u : OOB, 0u);
      list[lane] = k < cn ? ev : NOEDGE;
      agg_dma(rh, agg_voff(list[il], il, p), agg_voff(list[16u + il], il, p), ringa);
      asm volatile("s_waitcnt vmcnt(0)" ::: "memory");
      const u32x4 oh = agg_onehot(*(const u32x4*)(list + 4u * g), *(const u32x4*)(list + 16u + 4u * g), key);
#pragma unroll
      for (int dt = 0; dt < 8; ++dt) { const char* a = ring + (dt >> 1) * 1024 + ((dt & 1) ? vb1 : vb0); acc[dt] = mfma16(cat8(trread(a), trread(a + 4096)), asbf8(oh), acc[dt]); }
    }
  }
  agg_store(ws, G, lane, dinv, acc);
}
DI void agg_wave(char* __restrict__ ws, const uint16_t* __restrict__ deg_inv, char* L, unsigned wv, unsigned lane) {
  unsigned* list = (unsigned*)L;
  char* ring = L + LISTB;
  unsigned* tab = (unsigned*)ring;
  const unsigned ringa = rfl((unsigned)(size_t)ring);
  const unsigned G0 = TPW * wv;
  const rsrc_t re = mk_rsrc(ws + OFF_ENT, NCH * ECH * 4u), rh = agg_rsrc(ws), rc = mk_rsrc(ws + OFF_CELL, NG * 80u * 4u);
  unsigned c0[TPW], c1[TPW]; float dinv[TPW];
#pragma unroll
  for (int t = 0; t < TPW; ++t) {
    const unsigned G = G0 + t;
    c0[t] = bld4(rc, G < (unsigned)NG ? G * 320u + lane * 4u : OOB, 0u); c1[t] = bld4(rc, G < (unsigned)NG && lane < (unsigned)(NCH - 64) ? G * 320u + (64u + lane) * 4u : OOB, 0u);
    dinv[t] = __uint_as_float((unsigned)deg_inv[(G < (unsigned)NG ? 16u * G : 0u) + (lane & 15u)] << 16);
  }
  SB();
  unsigned endb[TPW], run = 0, len0 = 0, len1 = 0;
#pragma unroll
  for (int t = 0; t < TPW; ++t) {
    const unsigned n0 = c0[t] >> 16, n1 = c1[t] >> 16;
    const unsigned i0 = wscan_incl(n0, lane), t0 = __builtin_amdgcn_readlane(i0, 63), i1 = wscan_incl(n1, lane) + t0, total = __builtin_amdgcn_readlane(i1, 63);
    tab[lane * 16u + ((G0 + t) & 15u)] = run + i0 - n0 - len0;
    if (lane < (unsigned)(NCH - 64)) tab[(64u + lane) * 16u + ((G0 + t) & 15u)] = run + i1 - n1 - len1;
    len0 += n0; len1 += n1;
    run += (total + 31u) & ~31u; endb[t] = run >> 5;
  }
  STAMP(ws, 2048u + blockIdx.x * 4u + (threadIdx.x >> 6), 2);
  if (run + 128u > LISTB / 4u - 1u || __any((int)(len0 > 63u || len1 > 63u))) {
#pragma unroll 1
    for (unsigned G = G0; G < G0 + TPW && G < (unsigned)NG; ++G) agg_slow(ws, deg_inv, L, G, lane);
    return;
  }
  const unsigned s0 = (lane * ECH + (c0[0] & 0xffffu)) * 4u, s1 = ((64u + lane) * ECH + (c1[0] & 0xffffu)) * 4u;
  for (unsigned k = lane; k < run + 128u; k += 64u) list[k] = NOEDGE;
  const unsigned lane4 = lane * 4u, tabv = ringa, lst4 = (unsigned)(size_t)L + lane4;
  const unsigned n = lane & 15u, g = lane >> 4, q4 = n >> 2, p = lane & 3u, iv = 4u * g + q4, il = lane >> 2;
  const unsigned vb0 = iv * 64u + 16u * ((p >> 1) ^ xf(iv)) + 8u * (p & 1u);
  const unsigned avb0 = ringa + vb0, avb1 = ringa + (vb0 ^ 32u), sw = 16u * (p ^ xf(il)), lane16 = lane * 16u;
  const unsigned la = (unsigned)(size_t)L, lpe = la + il * 4u, lpa = la + g * 16u;
  STAMP(ws, 2048u + blockIdx.x * 4u + (threadIdx.x >> 6), 3);
  const rsrc_t rw = mk_rsrc(ws, WS_USED);
  unsigned nb[TPW], st[TPW], key[TPW];
#pragma unroll
  for (int t = 0; t < TPW; ++t) {
    const unsigned G = G0 + t;
    key[t] = ((G & 15u) << 4) | n;
    nb[t] = rfl(endb[t] - (t ? endb[t - 1] : 0u));
    st[t] = rfl(G < (unsigned)NG ? OFF_AGG + G * 4096u : OOB);
  }
  asm volatile(
      "s_nop 4\n\t"
      "v_readlane_b32 s64, %[s0], 0\n\t"
      "v_readlane_b32 s65, %[s0], 1\n\t"
      "v_readlane_b32 s66, %[s0], 2\n\t"
      "v_readlane_b32 s67, %[s0], 3\n\t"
      "v_readlane_b32 s68, %[s0], 4\n\t"
      "v_readlane_b32 s69, %[s0], 5\n\t"
      "v_readlane_b32 s70, %[s0], 6\n\t"
      "buffer_load_dword v100, %[lane4], %[re], s64 offen\n\t"
      "v_readlane_b32 s71, %[s0], 7\n\t"
      "buffer_load_dword v101, %[lane4], %[re], s65 offen\n\t"
      "v_readlane_b32 s64, %[s0], 8\n\t"
      "buffer_load_dword v102, %[lane4], %[re], s66 offen\n\t"
      "v_readlane_b32 s65, %[s0], 9\n\t"
      "buffer_load_dword v103, %[lane4], %[re], s67 offen\n\t"
      "v_readlane_b32 s66, %[s0], 10\n\t"
      "buffer_load_dword v104, %[lane4], %[re], s68 offen\n\t"
      "v_readlane_b32 s67, %[s0], 11\n\t"
      "buffer_load_dword v105, %[lane4], %[re], s69 offen\n\t"
      "v_readlane_b32 s68, %[s0], 12\n\t"
      "buffer_load_dword v106, %[lane4], %[re], s70 offen\n\t"
      "v_readlane_b32 s69, %[s0], 13\n\t"
      "buffer_load_dword v107, %[lane4], %[re], s71 offen\n\t"
      "v_readlane_b32 s70, %[s0], 14\n\t"
      "buffer_load_dword v108, %[lane4], %[re], s64 offen\n\t"
      "v_readlane_b32 s71, %[s0], 15\n\t"
      "buffer_load_dword v109, %[lane4], %[re], s65 offen\n\t"
      "v_readlane_b32 s64, %[s0], 16\n\t"
      "buffer_load_dword v110, %[lane4], %[re], s66 offen\n\t"
      "v_readlane_b32 s65, %[s0], 17\n\t"
      "buffer_load_dword v111, %[lane4], %[re], s67 offen\n\t"
      "v_readlane_b32 s66, %[s0], 18\n\t"
      "buffer_load_dword v112, %[lane4], %[re], s68 offen\n\t"
      "v_readlane_b32 s67, %[s0], 19\n\t"
      "buffer_load_dword v113, %[lane4], %[re], s69 offen\n\t"
      "v_readlane_b32 s68, %[s0], 20\n\t"
      "buffer_load_dword v114, %[lane4], %[re], s70 offen\n\t"
      "v_readlane_b32 s69, %[s0], 21\n\t"
      "buffer_load_dword v115, %[lane4], %[re], s71 offen\n\t"
      "v_readlane_b32 s70, %[s0], 22\n\t"
      "buffer_load_dword v116, %[lane4], %[re], s64 offen\n\t"
      "v_readlane_b32 s71, %[s0], 23\n\t"
      "buffer_load_dword v117, %[lane4], %[re], s65 offen\n\t"
      "v_readlane_b32 s64, %[s0], 24\n\t"
      "buffer_load_dword v118, %[lane4], %[re], s66 offen\n\t"
      "v_readlane_b32 s65, %[s0], 25\n\t"
      "buffer_load_dword v119, %[lane4], %[re], s67 offen\n\t"
      "v_readlane_b32 s66, %[s0], 26\n\t"
      "buffer_load_dword v120, %[lane4], %[re], s68 offen\n\t"
      "v_readlane_b32 s67, %[s0], 27\n\t"
      "buffer_load_dword v121, %[lane4], %[re], s69 offen\n\t"
      "v_readlane_b32 s68, %[s0], 28\n\t"
      "buffer_load_dword v122, %[lane4], %[re], s70 offen\n\t"
      "v_readlane_b32 s69, %[s0], 29\n\t"
      "buffer_load_dword v123, %[lane4], %[re], s71 offen\n\t"
      "v_readlane_b32 s70, %[s0], 30\n\t"
      "buffer_load_dword v124, %[lane4], %[re], s64 offen\n\t"
      "v_readlane_b32 s71, %[s0], 31\n\t"
      "buffer_load_dword v125, %[lane4], %[re], s65 offen\n\t"
      "v_readlane_b32 s64, %[s0], 32\n\t"
      "buffer_load_dword v126, %[lane4], %[re], s66 offen\n\t"
      "v_readlane_b32 s65, %[s0], 33\n\t"
      "buffer_load_dword v127, %[lane4], %[re], s67 offen\n\t"
      "v_readlane_b32 s66, %[s0], 34\n\t"
      "buffer_load_dword v128, %[lane4], %[re], s68 offen\n\t"
      "v_readlane_b32 s67, %[s0], 35\n\t"
      "buffer_load_dword v129, %[lane4], %[re], s69 offen\n\t"
      "v_readlane_b32 s68, %[s0], 36\n\t"
      "buffer_load_dword v130, %[lane4], %[re], s70 offen\n\t"
      "v_readlane_b32 s69, %[s0], 37\n\t"
      "buffer_load_dword v131, %[lane4], %[re], s71 offen\n\t"
      "v_readlane_b32 s70, %[s0], 38\n\t"
      "buffer_load_dword v132, %[lane4], %[re], s64 offen\n\t"
      "v_readlane_b32 s71, %[s0], 39\n\t"
      "buffer_load_dword v133, %[lane4], %[re], s65 offen\n\t"
      "v_readlane_b32 s64, %[s0], 40\n\t"
      "buffer_load_dword v134, %[lane4], %[re], s66 offen\n\t"
      "v_readlane_b32 s65, %[s0], 41\n\t"
      "buffer_load_dword v135, %[lane4], %[re], s67 offen\n\t"
      "v_readlane_b32 s66, %[s0], 42\n\t"
      "buffer_load_dword v136, %[lane4], %[re], s68 offen\n\t"
      "v_readlane_b32 s67, %[s0], 43\n\t"
      "buffer_load_dword v137, %[lane4], %[re], s69 offen\n\t"
      "v_readlane_b32 s68, %[s0], 44\n\t"
      "buffer_load_dword v138, %[lane4], %[re], s70 offen\n\t"
      "v_readlane_b32 s69, %[s0], 45\n\t"
      "buffer_load_dword v139, %[lane4], %[re], s71 offen\n\t"
      "v_readlane_b32 s70, %[s0], 46\n\t"
      "buffer_load_dword v140, %[lane4], %[re], s64 offen\n\t"
      "v_readlane_b32 s71, %[s0], 47\n\t"
      "buffer_load_dword v141, %[lane4], %[re], s65 offen\n\t"
      "v_readlane_b32 s64, %[s0], 48\n\t"
      "buffer_load_dword v142, %[lane4], %[re], s66 offen\n\t"
      "v_readlane_b32 s65, %[s0], 49\n\t"
      "buffer_load_dword v143, %[lane4], %[re], s67 offen\n\t"
      "v_readlane_b32 s66, %[s0], 50\n\t"
      "buffer_load_dword v144, %[lane4], %[re], s68 offen\n\t"
      "v_readlane_b32 s67, %[s0], 51\n\t"
      "buffer_load_dword v145, %[lane4], %[re], s69 offen\n\t"
      "v_readlane_b32 s68, %[s0], 52\n\t"
      "buffer_load_dword v146, %[lane4], %[re], s70 offen\n\t"
      "v_readlane_b32 s69, %[s0], 53\n\t"
      "buffer_load_dword v147, %[lane4], %[re], s71 offen\n\t"
      "v_readlane_b32 s70, %[s0], 54\n\t"
      "buffer_load_dword v148, %[lane4], %[re], s64 offen\n\t"
      "v_readlane_b32 s71, %[s0], 55\n\t"
      "buffer_load_dword v149, %[lane4], %[re], s65 offen\n\t"
      "v_readlane_b32 s64, %[s0], 56\n\t"
      "buffer_load_dword v150, %[lane4], %[re], s66 offen\n\t"
      "v_readlane_b32 s65, %[s0], 57\n\t"
      "buffer_load_dword v151, %[lane4], %[re], s67 offen\n\t"
      "v_readlane_b32 s66, %[s0], 58\n\t"
      "buffer_load_dword v152, %[lane4], %[re], s68 offen\n\t"
      "v_readlane_b32 s67, %[s0], 59\n\t"
      "buffer_load_dword v153, %[lane4], %[re], s69 offen\n\t"
      "v_readlane_b32 s68, %[s0], 60\n\t"
      "buffer_load_dword v154, %[lane4], %[re], s70 offen\n\t"
      "v_readlane_b32 s69, %[s0], 61\n\t"
      "buffer_load_dword v155, %[lane4], %[re], s71 offen\n\t"
      "v_readlane_b32 s70, %[s0], 62\n\t"
      "buffer_load_dword v156, %[lane4], %[re], s64 offen\n\t"
      "v_readlane_b32 s71, %[s0], 63\n\t"
      "buffer_load_dword v157, %[lane4], %[re], s65 offen\n\t"
      "v_readlane_b32 s64, %[s1], 0\n\t"
      "buffer_load_dword v158, %[lane4], %[re], s66 offen\n\t"
      "v_readlane_b32 s65, %[s1], 1\n\t"
      "buffer_load_dword v159, %[lane4], %[re], s67 offen\n\t"
      "v_readlane_b32 s66, %[s1], 2\n\t"
      "buffer_load_dword v160, %[lane4], %[re], s68 offen\n\t"
      "v_readlane_b32 s67, %[s1], 3\n\t"
      "buffer_load_dword v161, %[lane4], %[re], s69 offen\n\t"
      "v_readlane_b32 s68, %[s1], 4\n\t"
      "buffer_load_dword v162, %[lane4], %[re], s70 offen\n\t"
      "v_readlane_b32 s69, %[s1], 5\n\t"
      "buffer_load_dword v163, %[lane4], %[re], s71 offen\n\t"
      "v_readlane_b32 s70, %[s1], 6\n\t"
      "buffer_load_dword v164, %[lane4], %[re], s64 offen\n\t"
      "v_readlane_b32 s71, %[s1], 7\n\t"
      "buffer_load_dword v165, %[lane4], %[re], s65 offen\n\t"
      "v_readlane_b32 s64, %[s1], 8\n\t"
      "buffer_load_dword v166, %[lane4], %[re], s66 offen\n\t"
      "v_readlane_b32 s65, %[s1], 9\n\t"
      "buffer_load_dword v167, %[lane4], %[re], s67 offen\n\t"
      "v_readlane_b32 s66, %[s1], 10\n\t"
      "buffer_load_dword v168, %[lane4], %[re], s68 offen\n\t"
      "v_readlane_b32 s67, %[s1], 11\n\t"
      "buffer_load_dword v169, %[lane4], %[re], s69 offen\n\t"
      "v_readlane_b32 s68, %[s1], 12\n\t"
      "buffer_load_dword v170, %[lane4], %[re], s70 offen\n\t"
      "v_readlane_b32 s69, %[s1], 13\n\t"
      "buffer_load_dword v171, %[lane4], %[re], s71 offen\n\t"
      "v_readlane_b32 s70, %[s1], 14\n\t"
      "buffer_load_dword v172, %[lane4], %[re], s64 offen\n\t"
      "buffer_load_dword v173, %[lane4], %[re], s65 offen\n\t"
      "buffer_load_dword v174, %[lane4], %[re], s66 offen\n\t"
      "buffer_load_dword v175, %[lane4], %[re], s67 offen\n\t"
      "buffer_load_dword v176, %[lane4], %[re], s68 offen\n\t"
      "buffer_load_dword v177, %[lane4], %[re], s69 offen\n\t"
      "buffer_load_dword v178, %[lane4], %[re], s70 offen\n\t"
      "s_waitcnt vmcnt(63)\n\t"
      "v_lshrrev_b32_e32 v96, 2, v100\n\t"
      "v_and_or_b32 v97, v96, %[m3c], %[tabv]\n\t"
      "ds_read_b32 v180, v97 offset:0\n\t"
      "v_lshrrev_b32_e32 v96, 2, v101\n\t"
      "v_and_or_b32 v98, v96, %[m3c], %[tabv]\n\t"
      "ds_read_b32 v181, v98 offset:64\n\t"
      "v_lshrrev_b32_e32 v96, 2, v102\n\t"
      "v_and_or_b32 v97, v96, %[m3c], %[tabv]\n\t"
      "ds_read_b32 v182, v97 offset:128\n\t"
      "v_lshrrev_b32_e32 v96, 2, v103\n\t"
      "v_and_or_b32 v98, v96, %[m3c], %[tabv]\n\t"
      "ds_read_b32 v183, v98 offset:192\n\t"
      "v_lshrrev_b32_e32 v96, 2, v104\n\t"
      "v_and_or_b32 v97, v96, %[m3c], %[tabv]\n\t"
      "ds_read_b32 v184, v97 offset:256\n\t"
      "v_lshrrev_b32_e32 v96, 2, v105\n\t"
      "v_and_or_b32 v98, v96, %[m3c], %[tabv]\n\t"
      "ds_read_b32 v185, v98 offset:320\n\t"
      "v_lshrrev_b32_e32 v96, 2, v106\n\t"
      "v_and_or_b32 v97, v96, %[m3c], %[tabv]\n\t"
      "ds_read_b32 v186, v97 offset:384\n\t"
      "v_lshrrev_b32_e32 v96, 2, v107\n\t"
      "v_and_or_b32 v98, v96, %[m3c], %[tabv]\n\t"
      "ds_read_b32 v187, v98 offset:448\n\t"
      "v_lshrrev_b32_e32 v96, 2, v108\n\t"
      "v_and_or_b32 v97, v96, %[m3c], %[tabv]\n\t"
      "ds_read_b32 v188, v97 offset:512\n\t"
      "v_lshrrev_b32_e32 v96, 2, v109\n\t"
      "v_and_or_b32 v98, v96, %[m3c], %[tabv]\n\t"
      "ds_read_b32 v189, v98 offset:576\n\t"
      "v_lshrrev_b32_e32 v96, 2, v110\n\t"
      "v_and_or_b32 v97, v96, %[m3c], %[tabv]\n\t"
      "ds_read_b32 v190, v97 offset:640\n\t"
      "v_lshrrev_b32_e32 v96, 2, v111\n\t"
      "v_and_or_b32 v98, v96, %[m3c], %[tabv]\n\t"
      "ds_read_b32 v191, v98 offset:704\n\t"
      "v_lshrrev_b32_e32 v96, 2, v112\n\t"
      "v_and_or_b32 v97, v96, %[m3c], %[tabv]\n\t"
      "ds_read_b32 v192, v97 offset:768\n\t"
      "v_lshrrev_b32_e32 v96, 2, v113\n\t"
      "v_and_or_b32 v98, v96, %[m3c], %[tabv]\n\t"
      "ds_read_b32 v193, v98 offset:832\n\t"
      "v_lshrrev_b32_e32 v96, 2, v114\n\t"
      "v_and_or_b32 v97, v96, %[m3c], %[tabv]\n\t"
      "ds_read_b32 v194, v97 offset:896\n\t"
      "v_lshrrev_b32_e32 v96, 2, v115\n\t"
      "v_and_or_b32 v98, v96, %[m3c], %[tabv]\n\t"
      "ds_read_b32 v195, v98 offset:960\n\t"
      "v_readlane_b32 s64, %[len0], 0\n\t"
      "v_readlane_b32 s65, %[len0], 1\n\t"
      "s_waitcnt lgkmcnt(0)\n\t"
      "v_lshl_add_u32 v96, v180, 2, %[lst4]\n\t"
      "s_bfm_b64 exec, s64, 0\n\t"
      "ds_write_b32 v96, v100\n\t"
      "s_mov_b64 exec, -1\n\t"
      "v_readlane_b32 s66, %[len0], 2\n\t"
      "v_lshl_add_u32 v97, v181, 2, %[lst4]\n\t"
      "s_bfm_b64 exec, s65, 0\n\t"
      "ds_write_b32 v97, v101\n\t"
      "s_mov_b64 exec, -1\n\t"
      "v_readlane_b32 s67, %[len0], 3\n\t"
      "v_lshl_add_u32 v96, v182, 2, %[lst4]\n\t"
      "s_bfm_b64 exec, s66, 0\n\t"
      "ds_write_b32 v96, v102\n\t"
      "s_mov_b64 exec, -1\n\t"
      "v_readlane_b32 s68, %[len0], 4\n\t"
      "v_lshl_add_u32 v97, v183, 2, %[lst4]\n\t"
      "s_bfm_b64 exec, s67, 0\n\t"
      "ds_write_b32 v97, v103\n\t"
      "s_mov_b64 exec, -1\n\t"
      "v_readlane_b32 s69, %[len0], 5\n\t"
      "v_lshl_add_u32 v96, v184, 2, %[lst4]\n\t"
      "s_bfm_b64 exec, s68, 0\n\t"
      "ds_write_b32 v96, v104\n\t"
      "s_mov_b64 exec, -1\n\t"
      "v_readlane_b32 s70, %[len0], 6\n\t"
      "v_lshl_add_u32 v97, v185, 2, %[lst4]\n\t"
      "s_bfm_b64 exec, s69, 0\n\t"
      "ds_write_b32 v97, v105\n\t"
      "s_mov_b64 exec, -1\n\t"
      "v_readlane_b32 s71, %[len0], 7\n\t"
      "v_lshl_add_u32 v96, v186, 2, %[lst4]\n\t"
      "s_bfm_b64 exec, s70, 0\n\t"
      "ds_write_b32 v96, v106\n\t"
      "s_mov_b64 exec, -1\n\t"
      "v_readlane_b32 s64, %[len0], 8\n\t"
      "v_lshl_add_u32 v97, v187, 2, %[lst4]\n\t"
      "s_bfm_b64 exec, s71, 0\n\t"
      "ds_write_b32 v97, v107\n\t"
      "s_mov_b64 exec, -1\n\t"
      "v_readlane_b32 s65, %[len0], 9\n\t"
      "v_lshl_add_u32 v96, v188, 2, %[lst4]\n\t"
      "s_bfm_b64 exec, s64, 0\n\t"
      "ds_write_b32 v96, v108\n\t"
      "s_mov_b64 exec, -1\n\t"
      "v_readlane_b32 s66, %[len0], 10\n\t"
      "v_lshl_add_u32 v97, v189, 2, %[lst4]\n\t"
      "s_bfm_b64 exec, s65, 0\n\t"
      "ds_write_b32 v97, v109\n\t"
      "s_mov_b64 exec, -1\n\t"
      "v_readlane_b32 s67, %[len0], 11\n\t"
      "v_lshl_add_u32 v96, v190, 2, %[lst4]\n\t"
      "s_bfm_b64 exec, s66, 0\n\t"
      "ds_write_b32 v96, v110\n\t"
      "s_mov_b64 exec, -1\n\t"
      "v_readlane_b32 s68, %[len0], 12\n\t"
      "v_lshl_add_u32 v97, v191, 2, %[lst4]\n\t"
      "s_bfm_b64 exec, s67, 0\n\t"
      "ds_write_b32 v97, v111\n\t"
      "s_mov_b64 exec, -1\n\t"
      "v_readlane_b32 s69, %[len0], 13\n\t"
      "v_lshl_add_u32 v96, v192, 2, %[lst4]\n\t"
      "s_bfm_b64 exec, s68, 0\n\t"
      "ds_write_b32 v96, v112\n\t"
      "s_mov_b64 exec, -1\n\t"
      "v_readlane_b32 s70, %[len0], 14\n\t"
      "v_lshl_add_u32 v97, v193, 2, %[lst4]\n\t"
      "s_bfm_b64 exec, s69, 0\n\t"
      "ds_write_b32 v97, v113\n\t"
      "s_mov_b64 exec, -1\n\t"
      "v_readlane_b32 s71, %[len0], 15\n\t"
      "v_lshl_add_u32 v96, v194, 2, %[lst4]\n\t"
      "s_bfm_b64 exec, s70, 0\n\t"
      "ds_write_b32 v96, v114\n\t"
      "s_mov_b64 exec, -1\n\t"
      "v_lshl_add_u32 v97, v195, 2, %[lst4]\n\t"
      "s_bfm_b64 exec, s71, 0\n\t"
      "ds_write_b32 v97, v115\n\t"
      "s_mov_b64 exec, -1\n\t"
      "s_waitcnt vmcnt(47)\n\t"
      "v_lshrrev_b32_e32 v96, 2, v116\n\t"
      "v_and_or_b32 v97, v96, %[m3c], %[tabv]\n\t"
      "ds_read_b32 v180, v97 offset:1024\n\t"
      "v_lshrrev_b32_e32 v96, 2, v117\n\t"
      "v_and_or_b32 v98, v96, %[m3c], %[tabv]\n\t"
      "ds_read_b32 v181, v98 offset:1088\n\t"
      "v_lshrrev_b32_e32 v96, 2, v118\n\t"
      "v_and_or_b32 v97, v96, %[m3c], %[tabv]\n\t"
      "ds_read_b32 v182, v97 offset:1152\n\t"
      "v_lshrrev_b32_e32 v96, 2, v119\n\t"
      "v_and_or_b32 v98, v96, %[m3c], %[tabv]\n\t"
      "ds_read_b32 v183, v98 offset:1216\n\t"
      "v_lshrrev_b32_e32 v96, 2, v120\n\t"
      "v_and_or_b32 v97, v96, %[m3c], %[tabv]\n\t"
      "ds_read_b32 v184, v97 offset:1280\n\t"
      "v_lshrrev_b32_e32 v96, 2, v121\n\t"
      "v_and_or_b32 v98, v96, %[m3c], %[tabv]\n\t"
      "ds_read_b32 v185, v98 offset:1344\n\t"
      "v_lshrrev_b32_e32 v96, 2, v122\n\t"
      "v_and_or_b32 v97, v96, %[m3c], %[tabv]\n\t"
      "ds_read_b32 v186, v97 offset:1408\n\t"
      "v_lshrrev_b32_e32 v96, 2, v123\n\t"
      "v_and_or_b32 v98, v96, %[m3c], %[tabv]\n\t"
      "ds_read_b32 v187, v98 offset:1472\n\t"
      "v_lshrrev_b32_e32 v96, 2, v124\n\t"
      "v_and_or_b32 v97, v96, %[m3c], %[tabv]\n\t"
      "ds_read_b32 v188, v97 offset:1536\n\t"
      "v_lshrrev_b32_e32 v96, 2, v125\n\t"
      "v_and_or_b32 v98, v96, %[m3c], %[tabv]\n\t"
      "ds_read_b32 v189, v98 offset:1600\n\t"
      "v_lshrrev_b32_e32 v96, 2, v126\n\t"
      "v_and_or_b32 v97, v96, %[m3c], %[tabv]\n\t"
      "ds_read_b32 v190, v97 offset:1664\n\t"
      "v_lshrrev_b32_e32 v96, 2, v127\n\t"
      "v_and_or_b32 v98, v96, %[m3c], %[tabv]\n\t"
      "ds_read_b32 v191, v98 offset:1728\n\t"
      "v_lshrrev_b32_e32 v96, 2, v128\n\t"
      "v_and_or_b32 v97, v96, %[m3c], %[tabv]\n\t"
      "ds_read_b32 v192, v97 offset:1792\n\t"
      "v_lshrrev_b32_e32 v96, 2, v129\n\t"
      "v_and_or_b32 v98, v96, %[m3c], %[tabv]\n\t"
      "ds_read_b32 v193, v98 offset:1856\n\t"
      "v_lshrrev_b32_e32 v96, 2, v130\n\t"
      "v_and_or_b32 v97, v96, %[m3c], %[tabv]\n\t"
      "ds_read_b32 v194, v97 offset:1920\n\t"
      "v_lshrrev_b32_e32 v96, 2, v131\n\t"
      "v_and_or_b32 v98, v96, %[m3c], %[tabv]\n\t"
      "ds_read_b32 v195, v98 offset:1984\n\t"
      "v_readlane_b32 s64, %[len0], 16\n\t"
      "v_readlane_b32 s65, %[len0], 17\n\t"
      "s_waitcnt lgkmcnt(0)\n\t"
      "v_lshl_add_u32 v96, v180, 2, %[lst4]\n\t"
      "s_bfm_b64 exec, s64, 0\n\t"
      "ds_write_b32 v96, v116\n\t"
      "s_mov_b64 exec, -1\n\t"
      "v_readlane_b32 s66, %[len0], 18\n\t"
      "v_lshl_add_u32 v97, v181, 2, %[lst4]\n\t"
      "s_bfm_b64 exec, s65, 0\n\t"
      "ds_write_b32 v97, v117\n\t"
      "s_mov_b64 exec, -1\n\t"
      "v_readlane_b32 s67, %[len0], 19\n\t"
      "v_lshl_add_u32 v96, v182, 2, %[lst4]\n\t"
      "s_bfm_b64 exec, s66, 0\n\t"
      "ds_write_b32 v96, v118\n\t"
      "s_mov_b64 exec, -1\n\t"
      "v_readlane_b32 s68, %[len0], 20\n\t"
      "v_lshl_add_u32 v97, v183, 2, %[lst4]\n\t"
      "s_bfm_b64 exec, s67, 0\n\t"
      "ds_write_b32 v97, v119\n\t"
      "s_mov_b64 exec, -1\n\t"
      "v_readlane_b32 s69, %[len0], 21\n\t"
      "v_lshl_add_u32 v96, v184, 2, %[lst4]\n\t"
      "s_bfm_b64 exec, s68, 0\n\t"
      "ds_write_b32 v96, v120\n\t"
      "s_mov_b64 exec, -1\n\t"
      "v_readlane_b32 s70, %[len0], 22\n\t"
      "v_lshl_add_u32 v97, v185, 2, %[lst4]\n\t"
      "s_bfm_b64 exec, s69, 0\n\t"
      "ds_write_b32 v97, v121\n\t"
      "s_mov_b64 exec, -1\n\t"
      "v_readlane_b32 s71, %[len0], 23\n\t"
      "v_lshl_add_u32 v96, v186, 2, %[lst4]\n\t"
      "s_bfm_b64 exec, s70, 0\n\t"
      "ds_write_b32 v96, v122\n\t"
      "s_mov_b64 exec, -1\n\t"
      "v_readlane_b32 s64, %[len0], 24\n\t"
      "v_lshl_add_u32 v97, v187, 2, %[lst4]\n\t"
      "s_bfm_b64 exec, s71, 0\n\t"
      "ds_write_b32 v97, v123\n\t"
      "s_mov_b64 exec, -1\n\t"
      "v_readlane_b32 s65, %[len0], 25\n\t"
      "v_lshl_add_u32 v96, v188, 2, %[lst4]\n\t"
      "s_bfm_b64 exec, s64, 0\n\t"
      "ds_write_b32 v96, v124\n\t"
      "s_mov_b64 exec, -1\n\t"
      "v_readlane_b32 s66, %[len0], 26\n\t"
      "v_lshl_add_u32 v97, v189, 2, %[lst4]\n\t"
      "s_bfm_b64 exec, s65, 0\n\t"
      "ds_write_b32 v97, v125\n\t"
      "s_mov_b64 exec, -1\n\t"
      "v_readlane_b32 s67, %[len0], 27\n\t"
      "v_lshl_add_u32 v96, v190, 2, %[lst4]\n\t"
      "s_bfm_b64 exec, s66, 0\n\t"
      "ds_write_b32 v96, v126\n\t"
      "s_mov_b64 exec, -1\n\t"
      "v_readlane_b32 s68, %[len0], 28\n\t"
      "v_lshl_add_u32 v97, v191, 2, %[lst4]\n\t"
      "s_bfm_b64 exec, s67, 0\n\t"
      "ds_write_b32 v97, v127\n\t"
      "s_mov_b64 exec, -1\n\t"
      "v_readlane_b32 s69, %[len0], 29\n\t"
      "v_lshl_add_u32 v96, v192, 2, %[lst4]\n\t"
      "s_bfm_b64 exec, s68, 0\n\t"
      "ds_write_b32 v96, v128\n\t"
      "s_mov_b64 exec, -1\n\t"
      "v_readlane_b32 s70, %[len0], 30\n\t"
      "v_lshl_add_u32 v97, v193, 2, %[lst4]\n\t"
      "s_bfm_b64 exec, s69, 0\n\t"
      "ds_write_b32 v97, v129\n\t"
      "s_mov_b64 exec, -1\n\t"
      "v_readlane_b32 s71, %[len0], 31\n\t"
      "v_lshl_add_u32 v96, v194, 2, %[lst4]\n\t"
      "s_bfm_b64 exec, s70, 0\n\t"
      "ds_write_b32 v96, v130\n\t"
      "s_mov_b64 exec, -1\n\t"
      "v_lshl_add_u32 v97, v195, 2, %[lst4]\n\t"
      "s_bfm_b64 exec, s71, 0\n\t"
      "ds_write_b32 v97, v131\n\t"
      "s_mov_b64 exec, -1\n\t"
      "s_waitcnt vmcnt(31)\n\t"
      "v_lshrrev_b32_e32 v96, 2, v132\n\t"
      "v_and_or_b32 v97, v96, %[m3c], %[tabv]\n\t"
      "ds_read_b32 v180, v97 offset:2048\n\t"
      "v_lshrrev_b32_e32 v96, 2, v133\n\t"
      "v_and_or_b32 v98, v96, %[m3c], %[tabv]\n\t"
      "ds_read_b32 v181, v98 offset:2112\n\t"
      "v_lshrrev_b32_e32 v96, 2, v134\n\t"
      "v_and_or_b32 v97, v96, %[m3c], %[tabv]\n\t"
      "ds_read_b32 v182, v97 offset:2176\n\t"
      "v_lshrrev_b32_e32 v96, 2, v135\n\t"
      "v_and_or_b32 v98, v96, %[m3c], %[tabv]\n\t"
      "ds_read_b32 v183, v98 offset:2240\n\t"
      "v_lshrrev_b32_e32 v96, 2, v136\n\t"
      "v_and_or_b32 v97, v96, %[m3c], %[tabv]\n\t"
      "ds_read_b32 v184, v97 offset:2304\n\t"
      "v_lshrrev_b32_e32 v96, 2, v137\n\t"
      "v_and_or_b32 v98, v96, %[m3c], %[tabv]\n\t"
      "ds_read_b32 v185, v98 offset:2368\n\t"
      "v_lshrrev_b32_e32 v96, 2, v138\n\t"
      "v_and_or_b32 v97, v96, %[m3c], %[tabv]\n\t"
      "ds_read_b32 v186, v97 offset:2432\n\t"
      "v_lshrrev_b32_e32 v96, 2, v139\n\t"
      "v_and_or_b32 v98, v96, %[m3c], %[tabv]\n\t"
      "ds_read_b32 v187, v98 offset:2496\n\t"
      "v_lshrrev_b32_e32 v96, 2, v140\n\t"
      "v_and_or_b32 v97, v96, %[m3c], %[tabv]\n\t"
      "ds_read_b32 v188, v97 offset:2560\n\t"
      "v_lshrrev_b32_e32 v96, 2, v141\n\t"
      "v_and_or_b32 v98, v96, %[m3c], %[tabv]\n\t"
      "ds_read_b32 v189, v98 offset:2624\n\t"
      "v_lshrrev_b32_e32 v96, 2, v142\n\t"
      "v_and_or_b32 v97, v96, %[m3c], %[tabv]\n\t"
      "ds_read_b32 v190, v97 offset:2688\n\t"
      "v_lshrrev_b32_e32 v96, 2, v143\n\t"
      "v_and_or_b32 v98, v96, %[m3c], %[tabv]\n\t"
      "ds_read_b32 v191, v98 offset:2752\n\t"
      "v_lshrrev_b32_e32 v96, 2, v144\n\t"
      "v_and_or_b32 v97, v96, %[m3c], %[tabv]\n\t"
      "ds_read_b32 v192, v97 offset:2816\n\t"
      "v_lshrrev_b32_e32 v96, 2, v145\n\t"
      "v_and_or_b32 v98, v96, %[m3c], %[tabv]\n\t"
      "ds_read_b32 v193, v98 offset:2880\n\t"
      "v_lshrrev_b32_e32 v96, 2, v146\n\t"
      "v_and_or_b32 v97, v96, %[m3c], %[tabv]\n\t"
      "ds_read_b32 v194, v97 offset:2944\n\t"
      "v_lshrrev_b32_e32 v96, 2, v147\n\t"
      "v_and_or_b32 v98, v96, %[m3c], %[tabv]\n\t"
      "ds_read_b32 v195, v98 offset:3008\n\t"
      "v_readlane_b32 s64, %[len0], 32\n\t"
      "v_readlane_b32 s65, %[len0], 33\n\t"
      "s_waitcnt lgkmcnt(0)\n\t"
      "v_lshl_add_u32 v96, v180, 2, %[lst4]\n\t"
      "s_bfm_b64 exec, s64, 0\n\t"
      "ds_write_b32 v96, v132\n\t"
      "s_mov_b64 exec, -1\n\t"
      "v_readlane_b32 s66, %[len0], 34\n\t"
      "v_lshl_add_u32 v97, v181, 2, %[lst4]\n\t"
      "s_bfm_b64 exec, s65, 0\n\t"
      "ds_write_b32 v97, v133\n\t"
      "s_mov_b64 exec, -1\n\t"
      "v_readlane_b32 s67, %[len0], 35\n\t"
      "v_lshl_add_u32 v96, v182, 2, %[lst4]\n\t"
      "s_bfm_b64 exec, s66, 0\n\t"
      "ds_write_b32 v96, v134\n\t"
      "s_mov_b64 exec, -1\n\t"
      "v_readlane_b32 s68, %[len0], 36\n\t"
      "v_lshl_add_u32 v97, v183, 2, %[lst4]\n\t"
      "s_bfm_b64 exec, s67, 0\n\t"
      "ds_write_b32 v97, v135\n\t"
      "s_mov_b64 exec, -1\n\t"
      "v_readlane_b32 s69, %[len0], 37\n\t"
      "v_lshl_add_u32 v96, v184, 2, %[lst4]\n\t"
      "s_bfm_b64 exec, s68, 0\n\t"
      "ds_write_b32 v96, v136\n\t"
      "s_mov_b64 exec, -1\n\t"
      "v_readlane_b32 s70, %[len0], 38\n\t"
      "v_lshl_add_u32 v97, v185, 2, %[lst4]\n\t"
      "s_bfm_b64 exec, s69, 0\n\t"
      "ds_write_b32 v97, v137\n\t"
      "s_mov_b64 exec, -1\n\t"
      "v_readlane_b32 s71, %[len0], 39\n\t"
      "v_lshl_add_u32 v96, v186, 2, %[lst4]\n\t"
      "s_bfm_b64 exec, s70, 0\n\t"
      "ds_write_b32 v96, v138\n\t"
      "s_mov_b64 exec, -1\n\t"
      "v_readlane_b32 s64, %[len0], 40\n\t"
      "v_lshl_add_u32 v97, v187, 2, %[lst4]\n\t"
      "s_bfm_b64 exec, s71, 0\n\t"
      "ds_write_b32 v97, v139\n\t"
      "s_mov_b64 exec, -1\n\t"
      "v_readlane_b32 s65, %[len0], 41\n\t"
      "v_lshl_add_u32 v96, v188, 2, %[lst4]\n\t"
      "s_bfm_b64 exec, s64, 0\n\t"
      "ds_write_b32 v96, v140\n\t"
      "s_mov_b64 exec, -1\n\t"
      "v_readlane_b32 s66, %[len0], 42\n\t"
      "v_lshl_add_u32 v97, v189, 2, %[lst4]\n\t"
      "s_bfm_b64 exec, s65, 0\n\t"
      "ds_write_b32 v97, v141\n\t"
      "s_mov_b64 exec, -1\n\t"
      "v_readlane_b32 s67, %[len0], 43\n\t"
      "v_lshl_add_u32 v96, v190, 2, %[lst4]\n\t"
      "s_bfm_b64 exec, s66, 0\n\t"
      "ds_write_b32 v96, v142\n\t"
      "s_mov_b64 exec, -1\n\t"
      "v_readlane_b32 s68, %[len0], 44\n\t"
      "v_lshl_add_u32 v97, v191, 2, %[lst4]\n\t"
      "s_bfm_b64 exec, s67, 0\n\t"
      "ds_write_b32 v97, v143\n\t"
      "s_mov_b64 exec, -1\n\t"
      "v_readlane_b32 s69, %[len0], 45\n\t"
      "v_lshl_add_u32 v96, v192, 2, %[lst4]\n\t"
      "s_bfm_b64 exec, s68, 0\n\t"
      "ds_write_b32 v96, v144\n\t"
      "s_mov_b64 exec, -1\n\t"
      "v_readlane_b32 s70, %[len0], 46\n\t"
      "v_lshl_add_u32 v97, v193, 2, %[lst4]\n\t"
      "s_bfm_b64 exec, s69, 0\n\t"
      "ds_write_b32 v97, v145\n\t"
      "s_mov_b64 exec, -1\n\t"
      "v_readlane_b32 s71, %[len0], 47\n\t"
      "v_lshl_add_u32 v96, v194, 2, %[lst4]\n\t"
      "s_bfm_b64 exec, s70, 0\n\t"
      "ds_write_b32 v96, v146\n\t"
      "s_mov_b64 exec, -1\n\t"
      "v_lshl_add_u32 v97, v195, 2, %[lst4]\n\t"
      "s_bfm_b64 exec, s71, 0\n\t"
      "ds_write_b32 v97, v147\n\t"
      "s_mov_b64 exec, -1\n\t"
      "s_waitcnt vmcnt(15)\n\t"
      "v_lshrrev_b32_e32 v96, 2, v148\n\t"
      "v_and_or_b32 v97, v96, %[m3c], %[tabv]\n\t"
      "ds_read_b32 v180, v97 offset:3072\n\t"
      "v_lshrrev_b32_e32 v96, 2, v149\n\t"
      "v_and_or_b32 v98, v96, %[m3c], %[tabv]\n\t"
      "ds_read_b32 v181, v98 offset:3136\n\t"
      "v_lshrrev_b32_e32 v96, 2, v150\n\t"
      "v_and_or_b32 v97, v96, %[m3c], %[tabv]\n\t"
      "ds_read_b32 v182, v97 offset:3200\n\t"
      "v_lshrrev_b32_e32 v96, 2, v151\n\t"
      "v_and_or_b32 v98, v96, %[m3c], %[tabv]\n\t"
      "ds_read_b32 v183, v98 offset:3264\n\t"
      "v_lshrrev_b32_e32 v96, 2, v152\n\t"
      "v_and_or_b32 v97, v96, %[m3c], %[tabv]\n\t"
      "ds_read_b32 v184, v97 offset:3328\n\t"
      "v_lshrrev_b32_e32 v96, 2, v153\n\t"
      "v_and_or_b32 v98, v96, %[m3c], %[tabv]\n\t"
      "ds_read_b32 v185, v98 offset:3392\n\t"
      "v_lshrrev_b32_e32 v96, 2, v154\n\t"
      "v_and_or_b32 v97, v96, %[m3c], %[tabv]\n\t"
      "ds_read_b32 v186, v97 offset:3456\n\t"
      "v_lshrrev_b32_e32 v96, 2, v155\n\t"
      "v_and_or_b32 v98, v96, %[m3c], %[tabv]\n\t"
      "ds_read_b32 v187, v98 offset:3520\n\t"
      "v_lshrrev_b32_e32 v96, 2, v156\n\t"
      "v_and_or_b32 v97, v96, %[m3c], %[tabv]\n\t"
      "ds_read_b32 v188, v97 offset:3584\n\t"
      "v_lshrrev_b32_e32 v96, 2, v157\n\t"
      "v_and_or_b32 v98, v96, %[m3c], %[tabv]\n\t"
      "ds_read_b32 v189, v98 offset:3648\n\t"
      "v_lshrrev_b32_e32 v96, 2, v158\n\t"
      "v_and_or_b32 v97, v96, %[m3c], %[tabv]\n\t"
      "ds_read_b32 v190, v97 offset:3712\n\t"
      "v_lshrrev_b32_e32 v96, 2, v159\n\t"
      "v_and_or_b32 v98, v96, %[m3c], %[tabv]\n\t"
      "ds_read_b32 v191, v98 offset:3776\n\t"
      "v_lshrrev_b32_e32 v96, 2, v160\n\t"
      "v_and_or_b32 v97, v96, %[m3c], %[tabv]\n\t"
      "ds_read_b32 v192, v97 offset:3840\n\t"
      "v_lshrrev_b32_e32 v96, 2, v161\n\t"
      "v_and_or_b32 v98, v96, %[m3c], %[tabv]\n\t"
      "ds_read_b32 v193, v98 offset:3904\n\t"
      "v_lshrrev_b32_e32 v96, 2, v162\n\t"
      "v_and_or_b32 v97, v96, %[m3c], %[tabv]\n\t"
      "ds_read_b32 v194, v97 offset:3968\n\t"
      "v_lshrrev_b32_e32 v96, 2, v163\n\t"
      "v_and_or_b32 v98, v96, %[m3c], %[tabv]\n\t"
      "ds_read_b32 v195, v98 offset:4032\n\t"
      "v_readlane_b32 s64, %[len0], 48\n\t"
      "v_readlane_b32 s65, %[len0], 49\n\t"
      "s_waitcnt lgkmcnt(0)\n\t"
      "v_lshl_add_u32 v96, v180, 2, %[lst4]\n\t"
      "s_bfm_b64 exec, s64, 0\n\t"
      "ds_write_b32 v96, v148\n\t"
      "s_mov_b64 exec, -1\n\t"
      "v_readlane_b32 s66, %[len0], 50\n\t"
      "v_lshl_add_u32 v97, v181, 2, %[lst4]\n\t"
      "s_bfm_b64 exec, s65, 0\n\t"
      "ds_write_b32 v97, v149\n\t"
      "s_mov_b64 exec, -1\n\t"
      "v_readlane_b32 s67, %[len0], 51\n\t"
      "v_lshl_add_u32 v96, v182, 2, %[lst4]\n\t"
      "s_bfm_b64 exec, s66, 0\n\t"
      "ds_write_b32 v96, v150\n\t"
      "s_mov_b64 exec, -1\n\t"
      "v_readlane_b32 s68, %[len0], 52\n\t"
      "v_lshl_add_u32 v97, v183, 2, %[lst4]\n\t"
      "s_bfm_b64 exec, s67, 0\n\t"
      "ds_write_b32 v97, v151\n\t"
      "s_mov_b64 exec, -1\n\t"
      "v_readlane_b32 s69, %[len0], 53\n\t"
      "v_lshl_add_u32 v96, v184, 2, %[lst4]\n\t"
      "s_bfm_b64 exec, s68, 0\n\t"
      "ds_write_b32 v96, v152\n\t"
      "s_mov_b64 exec, -1\n\t"
      "v_readlane_b32 s70, %[len0], 54\n\t"
      "v_lshl_add_u32 v97, v185, 2, %[lst4]\n\t"
      "s_bfm_b64 exec, s69, 0\n\t"
      "ds_write_b32 v97, v153\n\t"
      "s_mov_b64 exec, -1\n\t"
      "v_readlane_b32 s71, %[len0], 55\n\t"
      "v_lshl_add_u32 v96, v186, 2, %[lst4]\n\t"
      "s_bfm_b64 exec, s70, 0\n\t"
      "ds_write_b32 v96, v154\n\t"
      "s_mov_b64 exec, -1\n\t"
      "v_readlane_b32 s64, %[len0], 56\n\t"
      "v_lshl_add_u32 v97, v187, 2, %[lst4]\n\t"
      "s_bfm_b64 exec, s71, 0\n\t"
      "ds_write_b32 v97, v155\n\t"
      "s_mov_b64 exec, -1\n\t"
      "v_readlane_b32 s65, %[len0], 57\n\t"
      "v_lshl_add_u32 v96, v188, 2, %[lst4]\n\t"
      "s_bfm_b64 exec, s64, 0\n\t"
      "ds_write_b32 v96, v156\n\t"
      "s_mov_b64 exec, -1\n\t"
      "v_readlane_b32 s66, %[len0], 58\n\t"
      "v_lshl_add_u32 v97, v189, 2, %[lst4]\n\t"
      "s_bfm_b64 exec, s65, 0\n\t"
      "ds_write_b32 v97, v157\n\t"
      "s_mov_b64 exec, -1\n\t"
      "v_readlane_b32 s67, %[len0], 59\n\t"
      "v_lshl_add_u32 v96, v190, 2, %[lst4]\n\t"
      "s_bfm_b64 exec, s66, 0\n\t"
      "ds_write_b32 v96, v158\n\t"
      "s_mov_b64 exec, -1\n\t"
      "v_readlane_b32 s68, %[len0], 60\n\t"
      "v_lshl_add_u32 v97, v191, 2, %[lst4]\n\t"
      "s_bfm_b64 exec, s67, 0\n\t"
      "ds_write_b32 v97, v159\n\t"
      "s_mov_b64 exec, -1\n\t"
      "v_readlane_b32 s69, %[len0], 61\n\t"
      "v_lshl_add_u32 v96, v192, 2, %[lst4]\n\t"
      "s_bfm_b64 exec, s68, 0\n\t"
      "ds_write_b32 v96, v160\n\t"
      "s_mov_b64 exec, -1\n\t"
      "v_readlane_b32 s70, %[len0], 62\n\t"
      "v_lshl_add_u32 v97, v193, 2, %[lst4]\n\t"
      "s_bfm_b64 exec, s69, 0\n\t"
      "ds_write_b32 v97, v161\n\t"
      "s_mov_b64 exec, -1\n\t"
      "v_readlane_b32 s71, %[len0], 63\n\t"
      "v_lshl_add_u32 v96, v194, 2, %[lst4]\n\t"
      "s_bfm_b64 exec, s70, 0\n\t"
      "ds_write_b32 v96, v162\n\t"
      "s_mov_b64 exec, -1\n\t"
      "v_lshl_add_u32 v97, v195, 2, %[lst4]\n\t"
      "s_bfm_b64 exec, s71, 0\n\t"
      "ds_write_b32 v97, v163\n\t"
      "s_mov_b64 exec, -1\n\t"
      "s_waitcnt vmcnt(0)\n\t"
      "v_lshrrev_b32_e32 v96, 2, v164\n\t"
      "v_and_or_b32 v97, v96, %[m3c], %[tabv]\n\t"
      "ds_read_b32 v180, v97 offset:4096\n\t"
      "v_lshrrev_b32_e32 v96, 2, v165\n\t"
      "v_and_or_b32 v98, v96, %[m3c], %[tabv]\n\t"
      "ds_read_b32 v181, v98 offset:4160\n\t"
      "v_lshrrev_b32_e32 v96, 2, v166\n\t"
      "v_and_or_b32 v97, v96, %[m3c], %[tabv]\n\t"
      "ds_read_b32 v182, v97 offset:4224\n\t"
      "v_lshrrev_b32_e32 v96, 2, v167\n\t"
      "v_and_or_b32 v98, v96, %[m3c], %[tabv]\n\t"
      "ds_read_b32 v183, v98 offset:4288\n\t"
      "v_lshrrev_b32_e32 v96, 2, v168\n\t"
      "v_and_or_b32 v97, v96, %[m3c], %[tabv]\n\t"
      "ds_read_b32 v184, v97 offset:4352\n\t"
      "v_lshrrev_b32_e32 v96, 2, v169\n\t"
      "v_and_or_b32 v98, v96, %[m3c], %[tabv]\n\t"
      "ds_read_b32 v185, v98 offset:4416\n\t"
      "v_lshrrev_b32_e32 v96, 2, v170\n\t"
      "v_and_or_b32 v97, v96, %[m3c], %[tabv]\n\t"
      "ds_read_b32 v186, v97 offset:4480\n\t"
      "v_lshrrev_b32_e32 v96, 2, v171\n\t"
      "v_and_or_b32 v98, v96, %[m3c], %[tabv]\n\t"
      "ds_read_b32 v187, v98 offset:4544\n\t"
      "v_lshrrev_b32_e32 v96, 2, v172\n\t"
      "v_and_or_b32 v97, v96, %[m3c], %[tabv]\n\t"
      "ds_read_b32 v188, v97 offset:4608\n\t"
      "v_lshrrev_b32_e32 v96, 2, v173\n\t"
      "v_and_or_b32 v98, v96, %[m3c], %[tabv]\n\t"
      "ds_read_b32 v189, v98 offset:4672\n\t"
      "v_lshrrev_b32_e32 v96, 2, v174\n\t"
      "v_and_or_b32 v97, v96, %[m3c], %[tabv]\n\t"
      "ds_read_b32 v190, v97 offset:4736\n\t"
      "v_lshrrev_b32_e32 v96, 2, v175\n\t"
      "v_and_or_b32 v98, v96, %[m3c], %[tabv]\n\t"
      "ds_read_b32 v191, v98 offset:4800\n\t"
      "v_lshrrev_b32_e32 v96, 2, v176\n\t"
      "v_and_or_b32 v97, v96, %[m3c], %[tabv]\n\t"
      "ds_read_b32 v192, v97 offset:4864\n\t"
      "v_lshrrev_b32_e32 v96, 2, v177\n\t"
      "v_and_or_b32 v98, v96, %[m3c], %[tabv]\n\t"
      "ds_read_b32 v193, v98 offset:4928\n\t"
      "v_lshrrev_b32_e32 v96, 2, v178\n\t"
      "v_and_or_b32 v97, v96, %[m3c], %[tabv]\n\t"
      "ds_read_b32 v194, v97 offset:4992\n\t"
      "v_readlane_b32 s64, %[len1], 0\n\t"
      "v_readlane_b32 s65, %[len1], 1\n\t"
      "s_waitcnt lgkmcnt(0)\n\t"
      "v_lshl_add_u32 v96, v180, 2, %[lst4]\n\t"
      "s_bfm_b64 exec, s64, 0\n\t"
      "ds_write_b32 v96, v164\n\t"
      "s_mov_b64 exec, -1\n\t"
      "v_readlane_b32 s66, %[len1], 2\n\t"
      "v_lshl_add_u32 v97, v181, 2, %[lst4]\n\t"
      "s_bfm_b64 exec, s65, 0\n\t"
      "ds_write_b32 v97, v165\n\t"
      "s_mov_b64 exec, -1\n\t"
      "v_readlane_b32 s67, %[len1], 3\n\t"
      "v_lshl_add_u32 v96, v182, 2, %[lst4]\n\t"
      "s_bfm_b64 exec, s66, 0\n\t"
      "ds_write_b32 v96, v166\n\t"
      "s_mov_b64 exec, -1\n\t"
      "v_readlane_b32 s68, %[len1], 4\n\t"
      "v_lshl_add_u32 v97, v183, 2, %[lst4]\n\t"
      "s_bfm_b64 exec, s67, 0\n\t"
      "ds_write_b32 v97, v167\n\t"
      "s_mov_b64 exec, -1\n\t"
      "v_readlane_b32 s69, %[len1], 5\n\t"
      "v_lshl_add_u32 v96, v184, 2, %[lst4]\n\t"
      "s_bfm_b64 exec, s68, 0\n\t"
      "ds_write_b32 v96, v168\n\t"
      "s_mov_b64 exec, -1\n\t"
      "v_readlane_b32 s70, %[len1], 6\n\t"
      "v_lshl_add_u32 v97, v185, 2, %[lst4]\n\t"
      "s_bfm_b64 exec, s69, 0\n\t"
      "ds_write_b32 v97, v169\n\t"
      "s_mov_b64 exec, -1\n\t"
      "v_readlane_b32 s71, %[len1], 7\n\t"
      "v_lshl_add_u32 v96, v186, 2, %[lst4]\n\t"
      "s_bfm_b64 exec, s70, 0\n\t"
      "ds_write_b32 v96, v170\n\t"
      "s_mov_b64 exec, -1\n\t"
      "v_readlane_b32 s64, %[len1], 8\n\t"
      "v_lshl_add_u32 v97, v187, 2, %[lst4]\n\t"
      "s_bfm_b64 exec, s71, 0\n\t"
      "ds_write_b32 v97, v171\n\t"
      "s_mov_b64 exec, -1\n\t"
      "v_readlane_b32 s65, %[len1], 9\n\t"
      "v_lshl_add_u32 v96, v188, 2, %[lst4]\n\t"
      "s_bfm_b64 exec, s64, 0\n\t"
      "ds_write_b32 v96, v172\n\t"
      "s_mov_b64 exec, -1\n\t"
      "v_readlane_b32 s66, %[len1], 10\n\t"
      "v_lshl_add_u32 v97, v189, 2, %[lst4]\n\t"
      "s_bfm_b64 exec, s65, 0\n\t"
      "ds_write_b32 v97, v173\n\t"
      "s_mov_b64 exec, -1\n\t"
      "v_readlane_b32 s67, %[len1], 11\n\t"
      "v_lshl_add_u32 v96, v190, 2, %[lst4]\n\t"
      "s_bfm_b64 exec, s66, 0\n\t"
      "ds_write_b32 v96, v174\n\t"
      "s_mov_b64 exec, -1\n\t"
      "v_readlane_b32 s68, %[len1], 12\n\t"
      "v_lshl_add_u32 v97, v191, 2, %[lst4]\n\t"
      "s_bfm_b64 exec, s67, 0\n\t"
      "ds_write_b32 v97, v175\n\t"
      "s_mov_b64 exec, -1\n\t"
      "v_readlane_b32 s69, %[len1], 13\n\t"
      "v_lshl_add_u32 v96, v192, 2, %[lst4]\n\t"
      "s_bfm_b64 exec, s68, 0\n\t"
      "ds_write_b32 v96, v176\n\t"
      "s_mov_b64 exec, -1\n\t"
      "v_readlane_b32 s70, %[len1], 14\n\t"
      "v_lshl_add_u32 v97, v193, 2, %[lst4]\n\t"
      "s_bfm_b64 exec, s69, 0\n\t"
      "ds_write_b32 v97, v177\n\t"
      "s_mov_b64 exec, -1\n\t"
      "v_lshl_add_u32 v96, v194, 2, %[lst4]\n\t"
      "s_bfm_b64 exec, s70, 0\n\t"
      "ds_write_b32 v96, v178\n\t"
      "s_mov_b64 exec, -1\n\t"
      "v_mov_b32_e32 v0, 0\n\t"
      "v_mov_b32_e32 v1, 0\n\t"
      "v_mov_b32_e32 v2, 0\n\t"
      "v_mov_b32_e32 v3, 0\n\t"
      "v_mov_b32_e32 v4, 0\n\t"
      "v_mov_b32_e32 v5, 0\n\t"
      "v_mov_b32_e32 v6, 0\n\t"
      "v_mov_b32_e32 v7, 0\n\t"
      "v_mov_b32_e32 v8, 0\n\t"
      "v_mov_b32_e32 v9, 0\n\t"
      "v_mov_b32_e32 v10, 0\n\t"
      "v_mov_b32_e32 v11, 0\n\t"
      "v_mov_b32_e32 v12, 0\n\t"
      "v_mov_b32_e32 v13, 0\n\t"
      "v_mov_b32_e32 v14, 0\n\t"
      "v_mov_b32_e32 v15, 0\n\t"
      "v_mov_b32_e32 v16, 0\n\t"
      "v_mov_b32_e32 v17, 0\n\t"
      "v_mov_b32_e32 v18, 0\n\t"
      "v_mov_b32_e32 v19, 0\n\t"
      "v_mov_b32_e32 v20, 0\n\t"
      "v_mov_b32_e32 v21, 0\n\t"
      "v_mov_b32_e32 v22, 0\n\t"
      "v_mov_b32_e32 v23, 0\n\t"
      "v_mov_b32_e32 v24, 0\n\t"
      "v_mov_b32_e32 v25, 0\n\t"
      "v_mov_b32_e32 v26, 0\n\t"
      "v_mov_b32_e32 v27, 0\n\t"
      "v_mov_b32_e32 v28, 0\n\t"
      "v_mov_b32_e32 v29, 0\n\t"
      "v_mov_b32_e32 v30, 0\n\t"
      "v_mov_b32_e32 v31, 0\n\t"
      "v_mov_b32_e32 v90, %[lpe]\n\t"
      "v_mov_b32_e32 v91, %[lpa]\n\t"
      "s_mov_b32 s60, 0\n\t"
      "ds_read2_b32 v[72:73], v90 offset1:16\n\t"
      "v_add_u32_e32 v90, 0x80, v90\n\t"
      "s_mov_b32 s61, 0\n\t"
      "s_waitcnt lgkmcnt(0)\n\t"
      "v_and_or_b32 v74, v72, %[msk], %[sw]\n\t"
      "v_and_or_b32 v75, v73, %[msk], %[sw]\n\t"
      "s_add_u32 m0, %[ringa], s61\n\t"
      "v_add_u32_e32 v76, s60, %[vb0]\n\t"
      "buffer_load_dwordx4 v74, %[rh], %[so0] offen lds\n\t"
      "buffer_load_dwordx4 v74, %[rh], %[so1] offen offset:1024 lds\n\t"
      "buffer_load_dwordx4 v74, %[rh], %[so2] offen offset:2048 lds\n\t"
      "buffer_load_dwordx4 v74, %[rh], %[so3] offen offset:3072 lds\n\t"
      "s_add_u32 m0, m0, 0x1000\n\t"
      "v_add_u32_e32 v77, s60, %[vb1]\n\t"
      "buffer_load_dwordx4 v75, %[rh], %[so0] offen lds\n\t"
      "buffer_load_dwordx4 v75, %[rh], %[so1] offen offset:1024 lds\n\t"
      "buffer_load_dwordx4 v75, %[rh], %[so2] offen offset:2048 lds\n\t"
      "buffer_load_dwordx4 v75, %[rh], %[so3] offen offset:3072 lds\n\t"
      "ds_read2_b32 v[72:73], v90 offset1:16\n\t"
      "v_add_u32_e32 v90, 0x80, v90\n\t"
      "s_mov_b32 s61, 8192\n\t"
      "s_waitcnt lgkmcnt(0)\n\t"
      "v_and_or_b32 v74, v72, %[msk], %[sw]\n\t"
      "v_and_or_b32 v75, v73, %[msk], %[sw]\n\t"
      "s_add_u32 m0, %[ringa], s61\n\t"
      "v_add_u32_e32 v76, s60, %[vb0]\n\t"
      "buffer_load_dwordx4 v74, %[rh], %[so0] offen lds\n\t"
      "buffer_load_dwordx4 v74, %[rh], %[so1] offen offset:1024 lds\n\t"
      "buffer_load_dwordx4 v74, %[rh], %[so2] offen offset:2048 lds\n\t"
      "buffer_load_dwordx4 v74, %[rh], %[so3] offen offset:3072 lds\n\t"
      "s_add_u32 m0, m0, 0x1000\n\t"
      "v_add_u32_e32 v77, s60, %[vb1]\n\t"
      "buffer_load_dwordx4 v75, %[rh], %[so0] offen lds\n\t"
      "buffer_load_dwordx4 v75, %[rh], %[so1] offen offset:1024 lds\n\t"
      "buffer_load_dwordx4 v75, %[rh], %[so2] offen offset:2048 lds\n\t"
      "buffer_load_dwordx4 v75, %[rh], %[so3] offen offset:3072 lds\n\t"
      "ds_read2_b32 v[72:73], v90 offset1:16\n\t"
      "ds_read_b128 v[64:67], v91\n\t"
      "ds_read_b128 v[68:71], v91 offset:64\n\t"
      "v_add_u32_e32 v90, 0x80, v90\n\t"
      "v_add_u32_e32 v91, 0x80, v91\n\t"
      "s_mov_b32 s61, 0x4000\n\t"
      "s_waitcnt lgkmcnt(0)\n\t"
      "s_mov_b32 s62, %[nb0]\n\t"
      "s_cmp_eq_u32 s62, 0\n\t"
      "s_cbranch_scc1 L_agg_done0_%=\n\t"
      "L_agg_loop0_%=:\n\t"
      "v_and_or_b32 v74, v72, %[msk], %[sw]\n\t"
      "v_and_or_b32 v75, v73, %[msk], %[sw]\n\t"
      "s_add_u32 m0, %[ringa], s61\n\t"
      "v_add_u32_e32 v76, s60, %[vb0]\n\t"
      "buffer_load_dwordx4 v74, %[rh], %[so0] offen lds\n\t"
      "buffer_load_dwordx4 v74, %[rh], %[so1] offen offset:1024 lds\n\t"
      "buffer_load_dwordx4 v74, %[rh], %[so2] offen offset:2048 lds\n\t"
      "buffer_load_dwordx4 v74, %[rh], %[so3] offen offset:3072 lds\n\t"
      "s_add_u32 m0, m0, 0x1000\n\t"
      "v_add_u32_e32 v77, s60, %[vb1]\n\t"
      "buffer_load_dwordx4 v75, %[rh], %[so0] offen lds\n\t"
      "buffer_load_dwordx4 v75, %[rh], %[so1] offen offset:1024 lds\n\t"
      "buffer_load_dwordx4 v75, %[rh], %[so2] offen offset:2048 lds\n\t"
      "buffer_load_dwordx4 v75, %[rh], %[so3] offen offset:3072 lds\n\t"
      "v_cmp_eq_u32_sdwa s[84:85], v64, %[key0] src0_sel:BYTE_0 src1_sel:DWORD\n\t"
      "v_cmp_eq_u32_sdwa s[86:87], v65, %[key0] src0_sel:BYTE_0 src1_sel:DWORD\n\t"
      "v_cmp_eq_u32_sdwa s[88:89], v66, %[key0] src0_sel:BYTE_0 src1_sel:DWORD\n\t"
      "v_cmp_eq_u32_sdwa s[90:91], v67, %[key0] src0_sel:BYTE_0 src1_sel:DWORD\n\t"
      "v_cmp_eq_u32_sdwa s[92:93], v68, %[key0] src0_sel:BYTE_0 src1_sel:DWORD\n\t"
      "v_cmp_eq_u32_sdwa s[94:95], v69, %[key0] src0_sel:BYTE_0 src1_sel:DWORD\n\t"
      "v_cmp_eq_u32_sdwa s[96:97], v70, %[key0] src0_sel:BYTE_0 src1_sel:DWORD\n\t"
      "v_cmp_eq_u32_sdwa s[98:99], v71, %[key0] src0_sel:BYTE_0 src1_sel:DWORD\n\t"
      "s_waitcnt vmcnt(16)\n\t"
      "ds_read_b64_tr_b16 v[32:33], v76 offset:0\n\t"
      "ds_read_b64_tr_b16 v[34:35], v76 offset:4096\n\t"
      "v_cndmask_b32_e64 v78, 0, %[one_lo], s[84:85]\n\t"
      "v_cndmask_b32_e64 v79, 0, %[one_hi], s[86:87]\n\t"
      "ds_read_b64_tr_b16 v[36:37], v77 offset:0\n\t"
      "ds_read_b64_tr_b16 v[38:39], v77 offset:4096\n\t"
      "v_cndmask_b32_e64 v80, 0, %[one_lo], s[88:89]\n\t"
      "v_cndmask_b32_e64 v81, 0, %[one_hi], s[90:91]\n\t"
      "ds_read_b64_tr_b16 v[40:41], v76 offset:1024\n\t"
      "ds_read_b64_tr_b16 v[42:43], v76 offset:5120\n\t"
      "v_cndmask_b32_e64 v82, 0, %[one_lo], s[92:93]\n\t"
      "v_cndmask_b32_e64 v83, 0, %[one_hi], s[94:95]\n\t"
      "ds_read_b64_tr_b16 v[44:45], v77 offset:1024\n\t"
      "ds_read_b64_tr_b16 v[46:47], v77 offset:5120\n\t"
      "v_cndmask_b32_e64 v84, 0, %[one_lo], s[96:97]\n\t"
      "v_cndmask_b32_e64 v85, 0, %[one_hi], s[98:99]\n\t"
      "ds_read_b64_tr_b16 v[48:49], v76 offset:2048\n\t"
      "ds_read_b64_tr_b16 v[50:51], v76 offset:6144\n\t"
      "ds_read_b64_tr_b16 v[52:53], v77 offset:2048\n\t"
      "ds_read_b64_tr_b16 v[54:55], v77 offset:6144\n\t"
      "ds_read_b64_tr_b16 v[56:57], v76 offset:3072\n\t"
      "ds_read_b64_tr_b16 v[58:59], v76 offset:7168\n\t"
      "ds_read_b64_tr_b16 v[60:61], v77 offset:3072\n\t"
      "ds_read_b64_tr_b16 v[62:63], v77 offset:7168\n\t"
      "v_or_b32_e32 v86, v78, v79\n\t"
      "v_or_b32_e32 v87, v80, v81\n\t"
      "v_or_b32_e32 v88, v82, v83\n\t"
      "v_or_b32_e32 v89, v84, v85\n\t"
      "ds_read2_b32 v[72:73], v90 offset1:16\n\t"
      "ds_read_b128 v[64:67], v91\n\t"
      "ds_read_b128 v[68:71], v91 offset:64\n\t"
      "v_add_u32_e32 v90, 0x80, v90\n\t"
      "v_add_u32_e32 v91, 0x80, v91\n\t"
      "s_mov_b32 s61, s60\n\t"
      "s_add_u32 s60, s60, 0x2000\n\t"
      "s_cmp_eq_u32 s60, 0x6000\n\t"
      "s_cselect_b32 s60, 0, s60\n\t"
      "s_waitcnt lgkmcnt(15)\n\t"
      "v_mfma_f32_16x16x32_bf16 v[0:3], v[32:35], v[86:89], v[0:3]\n\t"
      "v_mfma_f32_16x16x32_bf16 v[4:7], v[36:39], v[86:89], v[4:7]\n\t"
      "s_waitcnt lgkmcnt(11)\n\t"
      "v_mfma_f32_16x16x32_bf16 v[8:11], v[40:43], v[86:89], v[8:11]\n\t"
      "v_mfma_f32_16x16x32_bf16 v[12:15], v[44:47], v[86:89], v[12:15]\n\t"
      "s_waitcnt lgkmcnt(7)\n\t"
      "v_mfma_f32_16x16x32_bf16 v[16:19], v[48:51], v[86:89], v[16:19]\n\t"
      "v_mfma_f32_16x16x32_bf16 v[20:23], v[52:55], v[86:89], v[20:23]\n\t"
      "s_waitcnt lgkmcnt(3)\n\t"
      "v_mfma_f32_16x16x32_bf16 v[24:27], v[56:59], v[86:89], v[24:27]\n\t"
      "v_mfma_f32_16x16x32_bf16 v[28:31], v[60:63], v[86:89], v[28:31]\n\t"
      "s_sub_u32 s62, s62, 1\n\t"
      "s_waitcnt lgkmcnt(0)\n\t"
      "s_cmp_lg_u32 s62, 0\n\t"
      "s_cbranch_scc1 L_agg_loop0_%=\n\t"
      "s_nop 7\n\t"
      "L_agg_done0_%=:\n\t"
      "v_add_u32_e32 v92, %[st0], %[lane16]\n\t"
      "v_mul_f32_e32 v0, v0, %[dinv0]\n\t"
      "v_mul_f32_e32 v1, v1, %[dinv0]\n\t"
      "v_mul_f32_e32 v2, v2, %[dinv0]\n\t"
      "v_mul_f32_e32 v3, v3, %[dinv0]\n\t"
      "v_cvt_pk_bf16_f32 v32, v0, v1\n\t"
      "v_cvt_pk_bf16_f32 v33, v2, v3\n\t"
      "v_mul_f32_e32 v4, v4, %[dinv0]\n\t"
      "v_mul_f32_e32 v5, v5, %[dinv0]\n\t"
      "v_mul_f32_e32 v6, v6, %[dinv0]\n\t"
      "v_mul_f32_e32 v7, v7, %[dinv0]\n\t"
      "v_cvt_pk_bf16_f32 v34, v4, v5\n\t"
      "v_cvt_pk_bf16_f32 v35, v6, v7\n\t"
      "buffer_store_dwordx4 v[32:35], v92, %[rw], 0 offen sc1\n\t"
      "v_mul_f32_e32 v8, v8, %[dinv0]\n\t"
      "v_mul_f32_e32 v9, v9, %[dinv0]\n\t"
      "v_mul_f32_e32 v10, v10, %[dinv0]\n\t"
      "v_mul_f32_e32 v11, v11, %[dinv0]\n\t"
      "v_cvt_pk_bf16_f32 v36, v8, v9\n\t"
      "v_cvt_pk_bf16_f32 v37, v10, v11\n\t"
      "v_mul_f32_e32 v12, v12, %[dinv0]\n\t"
      "v_mul_f32_e32 v13, v13, %[dinv0]\n\t"
      "v_mul_f32_e32 v14, v14, %[dinv0]\n\t"
      "v_mul_f32_e32 v15, v15, %[dinv0]\n\t"
      "v_cvt_pk_bf16_f32 v38, v12, v13\n\t"
      "v_cvt_pk_bf16_f32 v39, v14, v15\n\t"
      "buffer_store_dwordx4 v[36:39], v92, %[rw], 0 offen offset:1024 sc1\n\t"
      "v_mul_f32_e32 v16, v16, %[dinv0]\n\t"
      "v_mul_f32_e32 v17, v17, %[dinv0]\n\t"
      "v_mul_f32_e32 v18, v18, %[dinv0]\n\t"
      "v_mul_f32_e32 v19, v19, %[dinv0]\n\t"
      "v_cvt_pk_bf16_f32 v40, v16, v17\n\t"
      "v_cvt_pk_bf16_f32 v41, v18, v19\n\t"
      "v_mul_f32_e32 v20, v20, %[dinv0]\n\t"
      "v_mul_f32_e32 v21, v21, %[dinv0]\n\t"
      "v_mul_f32_e32 v22, v22, %[dinv0]\n\t"
      "v_mul_f32_e32 v23, v23, %[dinv0]\n\t"
      "v_cvt_pk_bf16_f32 v42, v20, v21\n\t"
      "v_cvt_pk_bf16_f32 v43, v22, v23\n\t"
      "buffer_store_dwordx4 v[40:43], v92, %[rw], 0 offen offset:2048 sc1\n\t"
      "v_mul_f32_e32 v24, v24, %[dinv0]\n\t"
      "v_mul_f32_e32 v25, v25, %[dinv0]\n\t"
      "v_mul_f32_e32 v26, v26, %[dinv0]\n\t"
      "v_mul_f32_e32 v27, v27, %[dinv0]\n\t"
      "v_cvt_pk_bf16_f32 v44, v24, v25\n\t"
      "v_cvt_pk_bf16_f32 v45, v26, v27\n\t"
      "v_mul_f32_e32 v28, v28, %[dinv0]\n\t"
      "v_mul_f32_e32 v29, v29, %[dinv0]\n\t"
      "v_mul_f32_e32 v30, v30, %[dinv0]\n\t"
      "v_mul_f32_e32 v31, v31, %[dinv0]\n\t"
      "v_cvt_pk_bf16_f32 v46, v28, v29\n\t"
      "v_cvt_pk_bf16_f32 v47, v30, v31\n\t"
      "buffer_store_dwordx4 v[44:47], v92, %[rw], 0 offen offset:3072 sc1\n\t"
      "v_mov_b32_e32 v0, 0\n\t"
      "v_mov_b32_e32 v1, 0\n\t"
      "v_mov_b32_e32 v2, 0\n\t"
      "v_mov_b32_e32 v3, 0\n\t"
      "v_mov_b32_e32 v4, 0\n\t"
      "v_mov_b32_e32 v5, 0\n\t"
      "v_mov_b32_e32 v6, 0\n\t"
      "v_mov_b32_e32 v7, 0\n\t"
      "v_mov_b32_e32 v8, 0\n\t"
      "v_mov_b32_e32 v9, 0\n\t"
      "v_mov_b32_e32 v10, 0\n\t"
      "v_mov_b32_e32 v11, 0\n\t"
      "v_mov_b32_e32 v12, 0\n\t"
      "v_mov_b32_e32 v13, 0\n\t"
      "v_mov_b32_e32 v14, 0\n\t"
      "v_mov_b32_e32 v15, 0\n\t"
      "v_mov_b32_e32 v16, 0\n\t"
      "v_mov_b32_e32 v17, 0\n\t"
      "v_mov_b32_e32 v18, 0\n\t"
      "v_mov_b32_e32 v19, 0\n\t"
      "v_mov_b32_e32 v20, 0\n\t"
      "v_mov_b32_e32 v21, 0\n\t"
      "v_mov_b32_e32 v22, 0\n\t"
      "v_mov_b32_e32 v23, 0\n\t"
      "v_mov_b32_e32 v24, 0\n\t"
      "v_mov_b32_e32 v25, 0\n\t"
      "v_mov_b32_e32 v26, 0\n\t"
      "v_mov_b32_e32 v27, 0\n\t"
      "v_mov_b32_e32 v28, 0\n\t"
      "v_mov_b32_e32 v29, 0\n\t"
      "v_mov_b32_e32 v30, 0\n\t"
      "v_mov_b32_e32 v31, 0\n\t"
      "s_nop 1\n\t"
      "s_mov_b32 s62, %[nb1]\n\t"
      "s_cmp_eq_u32 s62, 0\n\t"
      "s_cbranch_scc1 L_agg_done1_%=\n\t"
      "L_agg_loop1_%=:\n\t"
      "v_and_or_b32 v74, v72, %[msk], %[sw]\n\t"
      "v_and_or_b32 v75, v73, %[msk], %[sw]\n\t"
      "s_add_u32 m0, %[ringa], s61\n\t"
      "v_add_u32_e32 v76, s60, %[vb0]\n\t"
      "buffer_load_dwordx4 v74, %[rh], %[so0] offen lds\n\t"
      "buffer_load_dwordx4 v74, %[rh], %[so1] offen offset:1024 lds\n\t"
      "buffer_load_dwordx4 v74, %[rh], %[so2] offen offset:2048 lds\n\t"
      "buffer_load_dwordx4 v74, %[rh], %[so3] offen offset:3072 lds\n\t"
      "s_add_u32 m0, m0, 0x1000\n\t"
      "v_add_u32_e32 v77, s60, %[vb1]\n\t"
      "buffer_load_dwordx4 v75, %[rh], %[so0] offen lds\n\t"
      "buffer_load_dwordx4 v75, %[rh], %[so1] offen offset:1024 lds\n\t"
      "buffer_load_dwordx4 v75, %[rh], %[so2] offen offset:2048 lds\n\t"
      "buffer_load_dwordx4 v75, %[rh], %[so3] offen offset:3072 lds\n\t"
      "v_cmp_eq_u32_sdwa s[84:85], v64, %[key1] src0_sel:BYTE_0 src1_sel:DWORD\n\t"
      "v_cmp_eq_u32_sdwa s[86:87], v65, %[key1] src0_sel:BYTE_0 src1_sel:DWORD\n\t"
      "v_cmp_eq_u32_sdwa s[88:89], v66, %[key1] src0_sel:BYTE_0 src1_sel:DWORD\n\t"
      "v_cmp_eq_u32_sdwa s[90:91], v67, %[key1] src0_sel:BYTE_0 src1_sel:DWORD\n\t"
      "v_cmp_eq_u32_sdwa s[92:93], v68, %[key1] src0_sel:BYTE_0 src1_sel:DWORD\n\t"
      "v_cmp_eq_u32_sdwa s[94:95], v69, %[key1] src0_sel:BYTE_0 src1_sel:DWORD\n\t"
      "v_cmp_eq_u32_sdwa s[96:97], v70, %[key1] src0_sel:BYTE_0 src1_sel:DWORD\n\t"
      "v_cmp_eq_u32_sdwa s[98:99], v71, %[key1] src0_sel:BYTE_0 src1_sel:DWORD\n\t"
      "s_waitcnt vmcnt(16)\n\t"
      "ds_read_b64_tr_b16 v[32:33], v76 offset:0\n\t"
      "ds_read_b64_tr_b16 v[34:35], v76 offset:4096\n\t"
      "v_cndmask_b32_e64 v78, 0, %[one_lo], s[84:85]\n\t"
      "v_cndmask_b32_e64 v79, 0, %[one_hi], s[86:87]\n\t"
      "ds_read_b64_tr_b16 v[36:37], v77 offset:0\n\t"
      "ds_read_b64_tr_b16 v[38:39], v77 offset:4096\n\t"
      "v_cndmask_b32_e64 v80, 0, %[one_lo], s[88:89]\n\t"
      "v_cndmask_b32_e64 v81, 0, %[one_hi], s[90:91]\n\t"
      "ds_read_b64_tr_b16 v[40:41], v76 offset:1024\n\t"
      "ds_read_b64_tr_b16 v[42:43], v76 offset:5120\n\t"
      "v_cndmask_b32_e64 v82, 0, %[one_lo], s[92:93]\n\t"
      "v_cndmask_b32_e64 v83, 0, %[one_hi], s[94:95]\n\t"
      "ds_read_b64_tr_b16 v[44:45], v77 offset:1024\n\t"
      "ds_read_b64_tr_b16 v[46:47], v77 offset:5120\n\t"
      "v_cndmask_b32_e64 v84, 0, %[one_lo], s[96:97]\n\t"
      "v_cndmask_b32_e64 v85, 0, %[one_hi], s[98:99]\n\t"
      "ds_read_b64_tr_b16 v[48:49], v76 offset:2048\n\t"
      "ds_read_b64_tr_b16 v[50:51], v76 offset:6144\n\t"
      "ds_read_b64_tr_b16 v[52:53], v77 offset:2048\n\t"
      "ds_read_b64_tr_b16 v[54:55], v77 offset:6144\n\t"
      "ds_read_b64_tr_b16 v[56:57], v76 offset:3072\n\t"
      "ds_read_b64_tr_b16 v[58:59], v76 offset:7168\n\t"
      "ds_read_b64_tr_b16 v[60:61], v77 offset:3072\n\t"
      "ds_read_b64_tr_b16 v[62:63], v77 offset:7168\n\t"
      "v_or_b32_e32 v86, v78, v79\n\t"
      "v_or_b32_e32 v87, v80, v81\n\t"
      "v_or_b32_e32 v88, v82, v83\n\t"
      "v_or_b32_e32 v89, v84, v85\n\t"
      "ds_read2_b32 v[72:73], v90 offset1:16\n\t"
      "ds_read_b128 v[64:67], v91\n\t"
      "ds_read_b128 v[68:71], v91 offset:64\n\t"
      "v_add_u32_e32 v90, 0x80, v90\n\t"
      "v_add_u32_e32 v91, 0x80, v91\n\t"
      "s_mov_b32 s61, s60\n\t"
      "s_add_u32 s60, s60, 0x2000\n\t"
      "s_cmp_eq_u32 s60, 0x6000\n\t"
      "s_cselect_b32 s60, 0, s60\n\t"
      "s_waitcnt lgkmcnt(15)\n\t"
      "v_mfma_f32_16x16x32_bf16 v[0:3], v[32:35], v[86:89], v[0:3]\n\t"
      "v_mfma_f32_16x16x32_bf16 v[4:7], v[36:39], v[86:89], v[4:7]\n\t"
      "s_waitcnt lgkmcnt(11)\n\t"
      "v_mfma_f32_16x16x32_bf16 v[8:11], v[40:43], v[86:89], v[8:11]\n\t"
      "v_mfma_f32_16x16x32_bf16 v[12:15], v[44:47], v[86:89], v[12:15]\n\t"
      "s_waitcnt lgkmcnt(7)\n\t"
      "v_mfma_f32_16x16x32_bf16 v[16:19], v[48:51], v[86:89], v[16:19]\n\t"
      "v_mfma_f32_16x16x32_bf16 v[20:23], v[52:55], v[86:89], v[20:23]\n\t"
      "s_waitcnt lgkmcnt(3)\n\t"
      "v_mfma_f32_16x16x32_bf16 v[24:27], v[56:59], v[86:89], v[24:27]\n\t"
      "v_mfma_f32_16x16x32_bf16 v[28:31], v[60:63], v[86:89], v[28:31]\n\t"
      "s_sub_u32 s62, s62, 1\n\t"
      "s_waitcnt lgkmcnt(0)\n\t"
      "s_cmp_lg_u32 s62, 0\n\t"
      "s_cbranch_scc1 L_agg_loop1_%=\n\t"
      "s_nop 7\n\t"
      "L_agg_done1_%=:\n\t"
      "v_add_u32_e32 v92, %[st1], %[lane16]\n\t"
      "v_mul_f32_e32 v0, v0, %[dinv1]\n\t"
      "v_mul_f32_e32 v1, v1, %[dinv1]\n\t"
      "v_mul_f32_e32 v2, v2, %[dinv1]\n\t"
      "v_mul_f32_e32 v3, v3, %[dinv1]\n\t"
      "v_cvt_pk_bf16_f32 v32, v0, v1\n\t"
      "v_cvt_pk_bf16_f32 v33, v2, v3\n\t"
      "v_mul_f32_e32 v4, v4, %[dinv1]\n\t"
      "v_mul_f32_e32 v5, v5, %[dinv1]\n\t"
      "v_mul_f32_e32 v6, v6, %[dinv1]\n\t"
      "v_mul_f32_e32 v7, v7, %[dinv1]\n\t"
      "v_cvt_pk_bf16_f32 v34, v4, v5\n\t"
      "v_cvt_pk_bf16_f32 v35, v6, v7\n\t"
      "buffer_store_dwordx4 v[32:35], v92, %[rw], 0 offen sc1\n\t"
      "v_mul_f32_e32 v8, v8, %[dinv1]\n\t"
      "v_mul_f32_e32 v9, v9, %[dinv1]\n\t"
      "v_mul_f32_e32 v10, v10, %[dinv1]\n\t"
      "v_mul_f32_e32 v11, v11, %[dinv1]\n\t"
      "v_cvt_pk_bf16_f32 v36, v8, v9\n\t"
      "v_cvt_pk_bf16_f32 v37, v10, v11\n\t"
      "v_mul_f32_e32 v12, v12, %[dinv1]\n\t"
      "v_mul_f32_e32 v13, v13, %[dinv1]\n\t"
      "v_mul_f32_e32 v14, v14, %[dinv1]\n\t"
      "v_mul_f32_e32 v15, v15, %[dinv1]\n\t"
      "v_cvt_pk_bf16_f32 v38, v12, v13\n\t"
      "v_cvt_pk_bf16_f32 v39, v14, v15\n\t"
      "buffer_store_dwordx4 v[36:39], v92, %[rw], 0 offen offset:1024 sc1\n\t"
      "v_mul_f32_e32 v16, v16, %[dinv1]\n\t"
      "v_mul_f32_e32 v17, v17, %[dinv1]\n\t"
      "v_mul_f32_e32 v18, v18, %[dinv1]\n\t"
      "v_mul_f32_e32 v19, v19, %[dinv1]\n\t"
      "v_cvt_pk_bf16_f32 v40, v16, v17\n\t"
      "v_cvt_pk_bf16_f32 v41, v18, v19\n\t"
      "v_mul_f32_e32 v20, v20, %[dinv1]\n\t"
      "v_mul_f32_e32 v21, v21, %[dinv1]\n\t"
      "v_mul_f32_e32 v22, v22, %[dinv1]\n\t"
      "v_mul_f32_e32 v23, v23, %[dinv1]\n\t"
      "v_cvt_pk_bf16_f32 v42, v20, v21\n\t"
      "v_cvt_pk_bf16_f32 v43, v22, v23\n\t"
      "buffer_store_dwordx4 v[40:43], v92, %[rw], 0 offen offset:2048 sc1\n\t"
      "v_mul_f32_e32 v24, v24, %[dinv1]\n\t"
      "v_mul_f32_e32 v25, v25, %[dinv1]\n\t"
      "v_mul_f32_e32 v26, v26, %[dinv1]\n\t"
      "v_mul_f32_e32 v27, v27, %[dinv1]\n\t"
      "v_cvt_pk_bf16_f32 v44, v24, v25\n\t"
      "v_cvt_pk_bf16_f32 v45, v26, v27\n\t"
      "v_mul_f32_e32 v28, v28, %[dinv1]\n\t"
      "v_mul_f32_e32 v29, v29, %[dinv1]\n\t"
      "v_mul_f32_e32 v30, v30, %[dinv1]\n\t"
      "v_mul_f32_e32 v31, v31, %[dinv1]\n\t"
      "v_cvt_pk_bf16_f32 v46, v28, v29\n\t"
      "v_cvt_pk_bf16_f32 v47, v30, v31\n\t"
      "buffer_store_dwordx4 v[44:47], v92, %[rw], 0 offen offset:3072 sc1\n\t"
      "v_mov_b32_e32 v0, 0\n\t"
      "v_mov_b32_e32 v1, 0\n\t"
      "v_mov_b32_e32 v2, 0\n\t"
      "v_mov_b32_e32 v3, 0\n\t"
      "v_mov_b32_e32 v4, 0\n\t"
      "v_mov_b32_e32 v5, 0\n\t"
      "v_mov_b32_e32 v6, 0\n\t"
      "v_mov_b32_e32 v7, 0\n\t"
      "v_mov_b32_e32 v8, 0\n\t"
      "v_mov_b32_e32 v9, 0\n\t"
      "v_mov_b32_e32 v10, 0\n\t"
      "v_mov_b32_e32 v11, 0\n\t"
      "v_mov_b32_e32 v12, 0\n\t"
      "v_mov_b32_e32 v13, 0\n\t"
      "v_mov_b32_e32 v14, 0\n\t"
      "v_mov_b32_e32 v15, 0\n\t"
      "v_mov_b32_e32 v16, 0\n\t"
      "v_mov_b32_e32 v17, 0\n\t"
      "v_mov_b32_e32 v18, 0\n\t"
      "v_mov_b32_e32 v19, 0\n\t"
      "v_mov_b32_e32 v20, 0\n\t"
      "v_mov_b32_e32 v21, 0\n\t"
      "v_mov_b32_e32 v22, 0\n\t"
      "v_mov_b32_e32 v23, 0\n\t"
      "v_mov_b32_e32 v24, 0\n\t"
      "v_mov_b32_e32 v25, 0\n\t"
      "v_mov_b32_e32 v26, 0\n\t"
      "v_mov_b32_e32 v27, 0\n\t"
      "v_mov_b32_e32 v28, 0\n\t"
      "v_mov_b32_e32 v29, 0\n\t"
      "v_mov_b32_e32 v30, 0\n\t"
      "v_mov_b32_e32 v31, 0\n\t"
      "s_nop 1\n\t"
      "s_mov_b32 s62, %[nb2]\n\t"
      "s_cmp_eq_u32 s62, 0\n\t"
      "s_cbranch_scc1 L_agg_done2_%=\n\t"
      "L_agg_loop2_%=:\n\t"
      "v_and_or_b32 v74, v72, %[msk], %[sw]\n\t"
      "v_and_or_b32 v75, v73, %[msk], %[sw]\n\t"
      "s_add_u32 m0, %[ringa], s61\n\t"
      "v_add_u32_e32 v76, s60, %[vb0]\n\t"
      "buffer_load_dwordx4 v74, %[rh], %[so0] offen lds\n\t"
      "buffer_load_dwordx4 v74, %[rh], %[so1] offen offset:1024 lds\n\t"
      "buffer_load_dwordx4 v74, %[rh], %[so2] offen offset:2048 lds\n\t"
      "buffer_load_dwordx4 v74, %[rh], %[so3] offen offset:3072 lds\n\t"
      "s_add_u32 m0, m0, 0x1000\n\t"
      "v_add_u32_e32 v77, s60, %[vb1]\n\t"
      "buffer_load_dwordx4 v75, %[rh], %[so0] offen lds\n\t"
      "buffer_load_dwordx4 v75, %[rh], %[so1] offen offset:1024 lds\n\t"
      "buffer_load_dwordx4 v75, %[rh], %[so2] offen offset:2048 lds\n\t"
      "buffer_load_dwordx4 v75, %[rh], %[so3] offen offset:3072 lds\n\t"
      "v_cmp_eq_u32_sdwa s[84:85], v64, %[key2] src0_sel:BYTE_0 src1_sel:DWORD\n\t"
      "v_cmp_eq_u32_sdwa s[86:87], v65, %[key2] src0_sel:BYTE_0 src1_sel:DWORD\n\t"
      "v_cmp_eq_u32_sdwa s[88:89], v66, %[key2] src0_sel:BYTE_0 src1_sel:DWORD\n\t"
      "v_cmp_eq_u32_sdwa s[90:91], v67, %[key2] src0_sel:BYTE_0 src1_sel:DWORD\n\t"
      "v_cmp_eq_u32_sdwa s[92:93], v68, %[key2] src0_sel:BYTE_0 src1_sel:DWORD\n\t"
      "v_cmp_eq_u32_sdwa s[94:95], v69, %[key2] src0_sel:BYTE_0 src1_sel:DWORD\n\t"
      "v_cmp_eq_u32_sdwa s[96:97], v70, %[key2] src0_sel:BYTE_0 src1_sel:DWORD\n\t"
      "v_cmp_eq_u32_sdwa s[98:99], v71, %[key2] src0_sel:BYTE_0 src1_sel:DWORD\n\t"
      "s_waitcnt vmcnt(16)\n\t"
      "ds_read_b64_tr_b16 v[32:33], v76 offset:0\n\t"
      "ds_read_b64_tr_b16 v[34:35], v76 offset:4096\n\t"
      "v_cndmask_b32_e64 v78, 0, %[one_lo], s[84:85]\n\t"
      "v_cndmask_b32_e64 v79, 0, %[one_hi], s[86:87]\n\t"
      "ds_read_b64_tr_b16 v[36:37], v77 offset:0\n\t"
      "ds_read_b64_tr_b16 v[38:39], v77 offset:4096\n\t"
      "v_cndmask_b32_e64 v80, 0, %[one_lo], s[88:89]\n\t"
      "v_cndmask_b32_e64 v81, 0, %[one_hi], s[90:91]\n\t"
      "ds_read_b64_tr_b16 v[40:41], v76 offset:1024\n\t"
      "ds_read_b64_tr_b16 v[42:43], v76 offset:5120\n\t"
      "v_cndmask_b32_e64 v82, 0, %[one_lo], s[92:93]\n\t"
      "v_cndmask_b32_e64 v83, 0, %[one_hi], s[94:95]\n\t"
      "ds_read_b64_tr_b16 v[44:45], v77 offset:1024\n\t"
      "ds_read_b64_tr_b16 v[46:47], v77 offset:5120\n\t"
      "v_cndmask_b32_e64 v84, 0, %[one_lo], s[96:97]\n\t"
      "v_cndmask_b32_e64 v85, 0, %[one_hi], s[98:99]\n\t"
      "ds_read_b64_tr_b16 v[48:49], v76 offset:2048\n\t"
      "ds_read_b64_tr_b16 v[50:51], v76 offset:6144\n\t"
      "ds_read_b64_tr_b16 v[52:53], v77 offset:2048\n\t"
      "ds_read_b64_tr_b16 v[54:55], v77 offset:6144\n\t"
      "ds_read_b64_tr_b16 v[56:57], v76 offset:3072\n\t"
      "ds_read_b64_tr_b16 v[58:59], v76 offset:7168\n\t"
      "ds_read_b64_tr_b16 v[60:61], v77 offset:3072\n\t"
      "ds_read_b64_tr_b16 v[62:63], v77 offset:7168\n\t"
      "v_or_b32_e32 v86, v78, v79\n\t"
      "v_or_b32_e32 v87, v80, v81\n\t"
      "v_or_b32_e32 v88, v82, v83\n\t"
      "v_or_b32_e32 v89, v84, v85\n\t"
      "ds_read2_b32 v[72:73], v90 offset1:16\n\t"
      "ds_read_b128 v[64:67], v91\n\t"
      "ds_read_b128 v[68:71], v91 offset:64\n\t"
      "v_add_u32_e32 v90, 0x80, v90\n\t"
      "v_add_u32_e32 v91, 0x80, v91\n\t"
      "s_mov_b32 s61, s60\n\t"
      "s_add_u32 s60, s60, 0x2000\n\t"
      "s_cmp_eq_u32 s60, 0x6000\n\t"
      "s_cselect_b32 s60, 0, s60\n\t"
      "s_waitcnt lgkmcnt(15)\n\t"
      "v_mfma_f32_16x16x32_bf16 v[0:3], v[32:35], v[86:89], v[0:3]\n\t"
      "v_mfma_f32_16x16x32_bf16 v[4:7], v[36:39], v[86:89], v[4:7]\n\t"
      "s_waitcnt lgkmcnt(11)\n\t"
      "v_mfma_f32_16x16x32_bf16 v[8:11], v[40:43], v[86:89], v[8:11]\n\t"
      "v_mfma_f32_16x16x32_bf16 v[12:15], v[44:47], v[86:89], v[12:15]\n\t"
      "s_waitcnt lgkmcnt(7)\n\t"
      "v_mfma_f32_16x16x32_bf16 v[16:19], v[48:51], v[86:89], v[16:19]\n\t"
      "v_mfma_f32_16x16x32_bf16 v[20:23], v[52:55], v[86:89], v[20:23]\n\t"
      "s_waitcnt lgkmcnt(3)\n\t"
      "v_mfma_f32_16x16x32_bf16 v[24:27], v[56:59], v[86:89], v[24:27]\n\t"
      "v_mfma_f32_16x16x32_bf16 v[28:31], v[60:63], v[86:89], v[28:31]\n\t"
      "s_sub_u32 s62, s62, 1\n\t"
      "s_waitcnt lgkmcnt(0)\n\t"
      "s_cmp_lg_u32 s62, 0\n\t"
      "s_cbranch_scc1 L_agg_loop2_%=\n\t"
      "s_nop 7\n\t"
      "L_agg_done2_%=:\n\t"
      "v_add_u32_e32 v92, %[st2], %[lane16]\n\t"
      "v_mul_f32_e32 v0, v0, %[dinv2]\n\t"
      "v_mul_f32_e32 v1, v1, %[dinv2]\n\t"
      "v_mul_f32_e32 v2, v2, %[dinv2]\n\t"
      "v_mul_f32_e32 v3, v3, %[dinv2]\n\t"
      "v_cvt_pk_bf16_f32 v32, v0, v1\n\t"
      "v_cvt_pk_bf16_f32 v33, v2, v3\n\t"
      "v_mul_f32_e32 v4, v4, %[dinv2]\n\t"
      "v_mul_f32_e32 v5, v5, %[dinv2]\n\t"
      "v_mul_f32_e32 v6, v6, %[dinv2]\n\t"
      "v_mul_f32_e32 v7, v7, %[dinv2]\n\t"
      "v_cvt_pk_bf16_f32 v34, v4, v5\n\t"
      "v_cvt_pk_bf16_f32 v35, v6, v7\n\t"
      "buffer_store_dwordx4 v[32:35], v92, %[rw], 0 offen sc1\n\t"
      "v_mul_f32_e32 v8, v8, %[dinv2]\n\t"
      "v_mul_f32_e32 v9, v9, %[dinv2]\n\t"
      "v_mul_f32_e32 v10, v10, %[dinv2]\n\t"
      "v_mul_f32_e32 v11, v11, %[dinv2]\n\t"
      "v_cvt_pk_bf16_f32 v36, v8, v9\n\t"
      "v_cvt_pk_bf16_f32 v37, v10, v11\n\t"
      "v_mul_f32_e32 v12, v12, %[dinv2]\n\t"
      "v_mul_f32_e32 v13, v13, %[dinv2]\n\t"
      "v_mul_f32_e32 v14, v14, %[dinv2]\n\t"
      "v_mul_f32_e32 v15, v15, %[dinv2]\n\t"
      "v_cvt_pk_bf16_f32 v38, v12, v13\n\t"
      "v_cvt_pk_bf16_f32 v39, v14, v15\n\t"
      "buffer_store_dwordx4 v[36:39], v92, %[rw], 0 offen offset:1024 sc1\n\t"
      "v_mul_f32_e32 v16, v16, %[dinv2]\n\t"
      "v_mul_f32_e32 v17, v17, %[dinv2]\n\t"
      "v_mul_f32_e32 v18, v18, %[dinv2]\n\t"
      "v_mul_f32_e32 v19, v19, %[dinv2]\n\t"
      "v_cvt_pk_bf16_f32 v40, v16, v17\n\t"
      "v_cvt_pk_bf16_f32 v41, v18, v19\n\t"
      "v_mul_f32_e32 v20, v20, %[dinv2]\n\t"
      "v_mul_f32_e32 v21, v21, %[dinv2]\n\t"
      "v_mul_f32_e32 v22, v22, %[dinv2]\n\t"
      "v_mul_f32_e32 v23, v23, %[dinv2]\n\t"
      "v_cvt_pk_bf16_f32 v42, v20, v21\n\t"
      "v_cvt_pk_bf16_f32 v43, v22, v23\n\t"
      "buffer_store_dwordx4 v[40:43], v92, %[rw], 0 offen offset:2048 sc1\n\t"
      "v_mul_f32_e32 v24, v24, %[dinv2]\n\t"
      "v_mul_f32_e32 v25, v25, %[dinv2]\n\t"
      "v_mul_f32_e32 v26, v26, %[dinv2]\n\t"
      "v_mul_f32_e32 v27, v27, %[dinv2]\n\t"
      "v_cvt_pk_bf16_f32 v44, v24, v25\n\t"
      "v_cvt_pk_bf16_f32 v45, v26, v27\n\t"
      "v_mul_f32_e32 v28, v28, %[dinv2]\n\t"
      "v_mul_f32_e32 v29, v29, %[dinv2]\n\t"
      "v_mul_f32_e32 v30, v30, %[dinv2]\n\t"
      "v_mul_f32_e32 v31, v31, %[dinv2]\n\t"
      "v_cvt_pk_bf16_f32 v46, v28, v29\n\t"
      "v_cvt_pk_bf16_f32 v47, v30, v31\n\t"
      "buffer_store_dwordx4 v[44:47], v92, %[rw], 0 offen offset:3072 sc1\n\t"
      "v_mov_b32_e32 v0, 0\n\t"
      "v_mov_b32_e32 v1, 0\n\t"
      "v_mov_b32_e32 v2, 0\n\t"
      "v_mov_b32_e32 v3, 0\n\t"
      "v_mov_b32_e32 v4, 0\n\t"
      "v_mov_b32_e32 v5, 0\n\t"
      "v_mov_b32_e32 v6, 0\n\t"
      "v_mov_b32_e32 v7, 0\n\t"
      "v_mov_b32_e32 v8, 0\n\t"
      "v_mov_b32_e32 v9, 0\n\t"
      "v_mov_b32_e32 v10, 0\n\t"
      "v_mov_b32_e32 v11, 0\n\t"
      "v_mov_b32_e32 v12, 0\n\t"
      "v_mov_b32_e32 v13, 0\n\t"
      "v_mov_b32_e32 v14, 0\n\t"
      "v_mov_b32_e32 v15, 0\n\t"
      "v_mov_b32_e32 v16, 0\n\t"
      "v_mov_b32_e32 v17, 0\n\t"
      "v_mov_b32_e32 v18, 0\n\t"
      "v_mov_b32_e32 v19, 0\n\t"
      "v_mov_b32_e32 v20, 0\n\t"
      "v_mov_b32_e32 v21, 0\n\t"
      "v_mov_b32_e32 v22, 0\n\t"
      "v_mov_b32_e32 v23, 0\n\t"
      "v_mov_b32_e32 v24, 0\n\t"
      "v_mov_b32_e32 v25, 0\n\t"
      "v_mov_b32_e32 v26, 0\n\t"
      "v_mov_b32_e32 v27, 0\n\t"
      "v_mov_b32_e32 v28, 0\n\t"
      "v_mov_b32_e32 v29, 0\n\t"
      "v_mov_b32_e32 v30, 0\n\t"
      "v_mov_b32_e32 v31, 0\n\t"
      "s_nop 1\n\t"
      "s_mov_b32 s62, %[nb3]\n\t"
      "s_cmp_eq_u32 s62, 0\n\t"
      "s_cbranch_scc1 L_agg_done3_%=\n\t"
      "L_agg_loop3_%=:\n\t"
      "v_and_or_b32 v74, v72, %[msk], %[sw]\n\t"
      "v_and_or_b32 v75, v73, %[msk], %[sw]\n\t"
      "s_add_u32 m0, %[ringa], s61\n\t"
      "v_add_u32_e32 v76, s60, %[vb0]\n\t"
      "buffer_load_dwordx4 v74, %[rh], %[so0] offen lds\n\t"
      "buffer_load_dwordx4 v74, %[rh], %[so1] offen offset:1024 lds\n\t"
      "buffer_load_dwordx4 v74, %[rh], %[so2] offen offset:2048 lds\n\t"
      "buffer_load_dwordx4 v74, %[rh], %[so3] offen offset:3072 lds\n\t"
      "s_add_u32 m0, m0, 0x1000\n\t"
      "v_add_u32_e32 v77, s60, %[vb1]\n\t"
      "buffer_load_dwordx4 v75, %[rh], %[so0] offen lds\n\t"
      "buffer_load_dwordx4 v75, %[rh], %[so1] offen offset:1024 lds\n\t"
      "buffer_load_dwordx4 v75, %[rh], %[so2] offen offset:2048 lds\n\t"
      "buffer_load_dwordx4 v75, %[rh], %[so3] offen offset:3072 lds\n\t"
      "v_cmp_eq_u32_sdwa s[84:85], v64, %[key3] src0_sel:BYTE_0 src1_sel:DWORD\n\t"
      "v_cmp_eq_u32_sdwa s[86:87], v65, %[key3] src0_sel:BYTE_0 src1_sel:DWORD\n\t"
      "v_cmp_eq_u32_sdwa s[88:89], v66, %[key3] src0_sel:BYTE_0 src1_sel:DWORD\n\t"
      "v_cmp_eq_u32_sdwa s[90:91], v67, %[key3] src0_sel:BYTE_0 src1_sel:DWORD\n\t"
      "v_cmp_eq_u32_sdwa s[92:93], v68, %[key3] src0_sel:BYTE_0 src1_sel:DWORD\n\t"
      "v_cmp_eq_u32_sdwa s[94:95], v69, %[key3] src0_sel:BYTE_0 src1_sel:DWORD\n\t"
      "v_cmp_eq_u32_sdwa s[96:97], v70, %[key3] src0_sel:BYTE_0 src1_sel:DWORD\n\t"
      "v_cmp_eq_u32_sdwa s[98:99], v71, %[key3] src0_sel:BYTE_0 src1_sel:DWORD\n\t"
      "s_waitcnt vmcnt(16)\n\t"
      "ds_read_b64_tr_b16 v[32:33], v76 offset:0\n\t"
      "ds_read_b64_tr_b16 v[34:35], v76 offset:4096\n\t"
      "v_cndmask_b32_e64 v78, 0, %[one_lo], s[84:85]\n\t"
      "v_cndmask_b32_e64 v79, 0, %[one_hi], s[86:87]\n\t"
      "ds_read_b64_tr_b16 v[36:37], v77 offset:0\n\t"
      "ds_read_b64_tr_b16 v[38:39], v77 offset:4096\n\t"
      "v_cndmask_b32_e64 v80, 0, %[one_lo], s[88:89]\n\t"
      "v_cndmask_b32_e64 v81, 0, %[one_hi], s[90:91]\n\t"
      "ds_read_b64_tr_b16 v[40:41], v76 offset:1024\n\t"
      "ds_read_b64_tr_b16 v[42:43], v76 offset:5120\n\t"
      "v_cndmask_b32_e64 v82, 0, %[one_lo], s[92:93]\n\t"
      "v_cndmask_b32_e64 v83, 0, %[one_hi], s[94:95]\n\t"
      "ds_read_b64_tr_b16 v[44:45], v77 offset:1024\n\t"
      "ds_read_b64_tr_b16 v[46:47], v77 offset:5120\n\t"
      "v_cndmask_b32_e64 v84, 0, %[one_lo], s[96:97]\n\t"
      "v_cndmask_b32_e64 v85, 0, %[one_hi], s[98:99]\n\t"
      "ds_read_b64_tr_b16 v[48:49], v76 offset:2048\n\t"
      "ds_read_b64_tr_b16 v[50:51], v76 offset:6144\n\t"
      "ds_read_b64_tr_b16 v[52:53], v77 offset:2048\n\t"
      "ds_read_b64_tr_b16 v[54:55], v77 offset:6144\n\t"
      "ds_read_b64_tr_b16 v[56:57], v76 offset:3072\n\t"
      "ds_read_b64_tr_b16 v[58:59], v76 offset:7168\n\t"
      "ds_read_b64_tr_b16 v[60:61], v77 offset:3072\n\t"
      "ds_read_b64_tr_b16 v[62:63], v77 offset:7168\n\t"
      "v_or_b32_e32 v86, v78, v79\n\t"
      "v_or_b32_e32 v87, v80, v81\n\t"
      "v_or_b32_e32 v88, v82, v83\n\t"
      "v_or_b32_e32 v89, v84, v85\n\t"
      "ds_read2_b32 v[72:73], v90 offset1:16\n\t"
      "ds_read_b128 v[64:67], v91\n\t"
      "ds_read_b128 v[68:71], v91 offset:64\n\t"
      "v_add_u32_e32 v90, 0x80, v90\n\t"
      "v_add_u32_e32 v91, 0x80, v91\n\t"
      "s_mov_b32 s61, s60\n\t"
      "s_add_u32 s60, s60, 0x2000\n\t"
      "s_cmp_eq_u32 s60, 0x6000\n\t"
      "s_cselect_b32 s60, 0, s60\n\t"
      "s_waitcnt lgkmcnt(15)\n\t"
      "v_mfma_f32_16x16x32_bf16 v[0:3], v[32:35], v[86:89], v[0:3]\n\t"
      "v_mfma_f32_16x16x32_bf16 v[4:7], v[36:39], v[86:89], v[4:7]\n\t"
      "s_waitcnt lgkmcnt(11)\n\t"
      "v_mfma_f32_16x16x32_bf16 v[8:11], v[40:43], v[86:89], v[8:11]\n\t"
      "v_mfma_f32_16x16x32_bf16 v[12:15], v[44:47], v[86:89], v[12:15]\n\t"
      "s_waitcnt lgkmcnt(7)\n\t"
      "v_mfma_f32_16x16x32_bf16 v[16:19], v[48:51], v[86:89], v[16:19]\n\t"
      "v_mfma_f32_16x16x32_bf16 v[20:23], v[52:55], v[86:89], v[20:23]\n\t"
      "s_waitcnt lgkmcnt(3)\n\t"
      "v_mfma_f32_16x16x32_bf16 v[24:27], v[56:59], v[86:89], v[24:27]\n\t"
      "v_mfma_f32_16x16x32_bf16 v[28:31], v[60:63], v[86:89], v[28:31]\n\t"
      "s_sub_u32 s62, s62, 1\n\t"
      "s_waitcnt lgkmcnt(0)\n\t"
      "s_cmp_lg_u32 s62, 0\n\t"
      "s_cbranch_scc1 L_agg_loop3_%=\n\t"
      "s_nop 7\n\t"
      "L_agg_done3_%=:\n\t"
      "v_add_u32_e32 v92, %[st3], %[lane16]\n\t"
      "v_mul_f32_e32 v0, v0, %[dinv3]\n\t"
      "v_mul_f32_e32 v1, v1, %[dinv3]\n\t"
      "v_mul_f32_e32 v2, v2, %[dinv3]\n\t"
      "v_mul_f32_e32 v3, v3, %[dinv3]\n\t"
      "v_cvt_pk_bf16_f32 v32, v0, v1\n\t"
      "v_cvt_pk_bf16_f32 v33, v2, v3\n\t"
      "v_mul_f32_e32 v4, v4, %[dinv3]\n\t"
      "v_mul_f32_e32 v5, v5, %[dinv3]\n\t"
      "v_mul_f32_e32 v6, v6, %[dinv3]\n\t"
      "v_mul_f32_e32 v7, v7, %[dinv3]\n\t"
      "v_cvt_pk_bf16_f32 v34, v4, v5\n\t"
      "v_cvt_pk_bf16_f32 v35, v6, v7\n\t"
      "buffer_store_dwordx4 v[32:35], v92, %[rw], 0 offen sc1\n\t"
      "v_mul_f32_e32 v8, v8, %[dinv3]\n\t"
      "v_mul_f32_e32 v9, v9, %[dinv3]\n\t"
      "v_mul_f32_e32 v10, v10, %[dinv3]\n\t"
      "v_mul_f32_e32 v11, v11, %[dinv3]\n\t"
      "v_cvt_pk_bf16_f32 v36, v8, v9\n\t"
      "v_cvt_pk_bf16_f32 v37, v10, v11\n\t"
      "v_mul_f32_e32 v12, v12, %[dinv3]\n\t"
      "v_mul_f32_e32 v13, v13, %[dinv3]\n\t"
      "v_mul_f32_e32 v14, v14, %[dinv3]\n\t"
      "v_mul_f32_e32 v15, v15, %[dinv3]\n\t"
      "v_cvt_pk_bf16_f32 v38, v12, v13\n\t"
      "v_cvt_pk_bf16_f32 v39, v14, v15\n\t"
      "buffer_store_dwordx4 v[36:39], v92, %[rw], 0 offen offset:1024 sc1\n\t"
      "v_mul_f32_e32 v16, v16, %[dinv3]\n\t"
      "v_mul_f32_e32 v17, v17, %[dinv3]\n\t"
      "v_mul_f32_e32 v18, v18, %[dinv3]\n\t"
      "v_mul_f32_e32 v19, v19, %[dinv3]\n\t"
      "v_cvt_pk_bf16_f32 v40, v16, v17\n\t"
      "v_cvt_pk_bf16_f32 v41, v18, v19\n\t"
      "v_mul_f32_e32 v20, v20, %[dinv3]\n\t"
      "v_mul_f32_e32 v21, v21, %[dinv3]\n\t"
      "v_mul_f32_e32 v22, v22, %[dinv3]\n\t"
      "v_mul_f32_e32 v23, v23, %[dinv3]\n\t"
      "v_cvt_pk_bf16_f32 v42, v20, v21\n\t"
      "v_cvt_pk_bf16_f32 v43, v22, v23\n\t"
      "buffer_store_dwordx4 v[40:43], v92, %[rw], 0 offen offset:2048 sc1\n\t"
      "v_mul_f32_e32 v24, v24, %[dinv3]\n\t"
      "v_mul_f32_e32 v25, v25, %[dinv3]\n\t"
      "v_mul_f32_e32 v26, v26, %[dinv3]\n\t"
      "v_mul_f32_e32 v27, v27, %[dinv3]\n\t"
      "v_cvt_pk_bf16_f32 v44, v24, v25\n\t"
      "v_cvt_pk_bf16_f32 v45, v26, v27\n\t"
      "v_mul_f32_e32 v28, v28, %[dinv3]\n\t"
      "v_mul_f32_e32 v29, v29, %[dinv3]\n\t"
      "v_mul_f32_e32 v30, v30, %[dinv3]\n\t"
      "v_mul_f32_e32 v31, v31, %[dinv3]\n\t"
      "v_cvt_pk_bf16_f32 v46, v28, v29\n\t"
      "v_cvt_pk_bf16_f32 v47, v30, v31\n\t"
      "buffer_store_dwordx4 v[44:47], v92, %[rw], 0 offen offset:3072 sc1\n\t"
      "v_mov_b32_e32 v0, 0\n\t"
      "v_mov_b32_e32 v1, 0\n\t"
      "v_mov_b32_e32 v2, 0\n\t"
      "v_mov_b32_e32 v3, 0\n\t"
      "v_mov_b32_e32 v4, 0\n\t"
      "v_mov_b32_e32 v5, 0\n\t"
      "v_mov_b32_e32 v6, 0\n\t"
      "v_mov_b32_e32 v7, 0\n\t"
      "v_mov_b32_e32 v8, 0\n\t"
      "v_mov_b32_e32 v9, 0\n\t"
      "v_mov_b32_e32 v10, 0\n\t"
      "v_mov_b32_e32 v11, 0\n\t"
      "v_mov_b32_e32 v12, 0\n\t"
      "v_mov_b32_e32 v13, 0\n\t"
      "v_mov_b32_e32 v14, 0\n\t"
      "v_mov_b32_e32 v15, 0\n\t"
      "v_mov_b32_e32 v16, 0\n\t"
      "v_mov_b32_e32 v17, 0\n\t"
      "v_mov_b32_e32 v18, 0\n\t"
      "v_mov_b32_e32 v19, 0\n\t"
      "v_mov_b32_e32 v20, 0\n\t"
      "v_mov_b32_e32 v21, 0\n\t"
      "v_mov_b32_e32 v22, 0\n\t"
      "v_mov_b32_e32 v23, 0\n\t"
      "v_mov_b32_e32 v24, 0\n\t"
      "v_mov_b32_e32 v25, 0\n\t"
      "v_mov_b32_e32 v26, 0\n\t"
      "v_mov_b32_e32 v27, 0\n\t"
      "v_mov_b32_e32 v28, 0\n\t"
      "v_mov_b32_e32 v29, 0\n\t"
      "v_mov_b32_e32 v30, 0\n\t"
      "v_mov_b32_e32 v31, 0\n\t"
      "s_nop 1\n\t"
      "s_mov_b32 s62, %[nb4]\n\t"
      "s_cmp_eq_u32 s62, 0\n\t"
      "s_cbranch_scc1 L_agg_done4_%=\n\t"
      "L_agg_loop4_%=:\n\t"
      "v_and_or_b32 v74, v72, %[msk], %[sw]\n\t"
      "v_and_or_b32 v75, v73, %[msk], %[sw]\n\t"
      "s_add_u32 m0, %[ringa], s61\n\t"
      "v_add_u32_e32 v76, s60, %[vb0]\n\t"
      "buffer_load_dwordx4 v74, %[rh], %[so0] offen lds\n\t"
      "buffer_load_dwordx4 v74, %[rh], %[so1] offen offset:1024 lds\n\t"
      "buffer_load_dwordx4 v74, %[rh], %[so2] offen offset:2048 lds\n\t"
      "buffer_load_dwordx4 v74, %[rh], %[so3] offen offset:3072 lds\n\t"
      "s_add_u32 m0, m0, 0x1000\n\t"
      "v_add_u32_e32 v77, s60, %[vb1]\n\t"
      "buffer_load_dwordx4 v75, %[rh], %[so0] offen lds\n\t"
      "buffer_load_dwordx4 v75, %[rh], %[so1] offen offset:1024 lds\n\t"
      "buffer_load_dwordx4 v75, %[rh], %[so2] offen offset:2048 lds\n\t"
      "buffer_load_dwordx4 v75, %[rh], %[so3] offen offset:3072 lds\n\t"
      "v_cmp_eq_u32_sdwa s[84:85], v64, %[key4] src0_sel:BYTE_0 src1_sel:DWORD\n\t"
      "v_cmp_eq_u32_sdwa s[86:87], v65, %[key4] src0_sel:BYTE_0 src1_sel:DWORD\n\t"
      "v_cmp_eq_u32_sdwa s[88:89], v66, %[key4] src0_sel:BYTE_0 src1_sel:DWORD\n\t"
      "v_cmp_eq_u32_sdwa s[90:91], v67, %[key4] src0_sel:BYTE_0 src1_sel:DWORD\n\t"
      "v_cmp_eq_u32_sdwa s[92:93], v68, %[key4] src0_sel:BYTE_0 src1_sel:DWORD\n\t"
      "v_cmp_eq_u32_sdwa s[94:95], v69, %[key4] src0_sel:BYTE_0 src1_sel:DWORD\n\t"
      "v_cmp_eq_u32_sdwa s[96:97], v70, %[key4] src0_sel:BYTE_0 src1_sel:DWORD\n\t"
      "v_cmp_eq_u32_sdwa s[98:99], v71, %[key4] src0_sel:BYTE_0 src1_sel:DWORD\n\t"
      "s_waitcnt vmcnt(16)\n\t"
      "ds_read_b64_tr_b16 v[32:33], v76 offset:0\n\t"
      "ds_read_b64_tr_b16 v[34:35], v76 offset:4096\n\t"
      "v_cndmask_b32_e64 v78, 0, %[one_lo], s[84:85]\n\t"
      "v_cndmask_b32_e64 v79, 0, %[one_hi], s[86:87]\n\t"
      "ds_read_b64_tr_b16 v[36:37], v77 offset:0\n\t"
      "ds_read_b64_tr_b16 v[38:39], v77 offset:4096\n\t"
      "v_cndmask_b32_e64 v80, 0, %[one_lo], s[88:89]\n\t"
      "v_cndmask_b32_e64 v81, 0, %[one_hi], s[90:91]\n\t"
      "ds_read_b64_tr_b16 v[40:41], v76 offset:1024\n\t"
      "ds_read_b64_tr_b16 v[42:43], v76 offset:5120\n\t"
      "v_cndmask_b32_e64 v82, 0, %[one_lo], s[92:93]\n\t"
      "v_cndmask_b32_e64 v83, 0, %[one_hi], s[94:95]\n\t"
      "ds_read_b64_tr_b16 v[44:45], v77 offset:1024\n\t"
      "ds_read_b64_tr_b16 v[46:47], v77 offset:5120\n\t"
      "v_cndmask_b32_e64 v84, 0, %[one_lo], s[96:97]\n\t"
      "v_cndmask_b32_e64 v85, 0, %[one_hi], s[98:99]\n\t"
      "ds_read_b64_tr_b16 v[48:49], v76 offset:2048\n\t"
      "ds_read_b64_tr_b16 v[50:51], v76 offset:6144\n\t"
      "ds_read_b64_tr_b16 v[52:53], v77 offset:2048\n\t"
      "ds_read_b64_tr_b16 v[54:55], v77 offset:6144\n\t"
      "ds_read_b64_tr_b16 v[56:57], v76 offset:3072\n\t"
      "ds_read_b64_tr_b16 v[58:59], v76 offset:7168\n\t"
      "ds_read_b64_tr_b16 v[60:61], v77 offset:3072\n\t"
      "ds_read_b64_tr_b16 v[62:63], v77 offset:7168\n\t"
      "v_or_b32_e32 v86, v78, v79\n\t"
      "v_or_b32_e32 v87, v80, v81\n\t"
      "v_or_b32_e32 v88, v82, v83\n\t"
      "v_or_b32_e32 v89, v84, v85\n\t"
      "ds_read2_b32 v[72:73], v90 offset1:16\n\t"
      "ds_read_b128 v[64:67], v91\n\t"
      "ds_read_b128 v[68:71], v91 offset:64\n\t"
      "v_add_u32_e32 v90, 0x80, v90\n\t"
      "v_add_u32_e32 v91, 0x80, v91\n\t"
      "s_mov_b32 s61, s60\n\t"
      "s_add_u32 s60, s60, 0x2000\n\t"
      "s_cmp_eq_u32 s60, 0x6000\n\t"
      "s_cselect_b32 s60, 0, s60\n\t"
      "s_waitcnt lgkmcnt(15)\n\t"
      "v_mfma_f32_16x16x32_bf16 v[0:3], v[32:35], v[86:89], v[0:3]\n\t"
      "v_mfma_f32_16x16x32_bf16 v[4:7], v[36:39], v[86:89], v[4:7]\n\t"
      "s_waitcnt lgkmcnt(11)\n\t"
      "v_mfma_f32_16x16x32_bf16 v[8:11], v[40:43], v[86:89], v[8:11]\n\t"
      "v_mfma_f32_16x16x32_bf16 v[12:15], v[44:47], v[86:89], v[12:15]\n\t"
      "s_waitcnt lgkmcnt(7)\n\t"
      "v_mfma_f32_16x16x32_bf16 v[16:19], v[48:51], v[86:89], v[16:19]\n\t"
      "v_mfma_f32_16x16x32_bf16 v[20:23], v[52:55], v[86:89], v[20:23]\n\t"
      "s_waitcnt lgkmcnt(3)\n\t"
      "v_mfma_f32_16x16x32_bf16 v[24:27], v[56:59], v[86:89], v[24:27]\n\t"
      "v_mfma_f32_16x16x32_bf16 v[28:31], v[60:63], v[86:89], v[28:31]\n\t"
      "s_sub_u32 s62, s62, 1\n\t"
      "s_waitcnt lgkmcnt(0)\n\t"
      "s_cmp_lg_u32 s62, 0\n\t"
      "s_cbranch_scc1 L_agg_loop4_%=\n\t"
      "s_nop 7\n\t"
      "L_agg_done4_%=:\n\t"
      "v_add_u32_e32 v92, %[st4], %[lane16]\n\t"
      "v_mul_f32_e32 v0, v0, %[dinv4]\n\t"
      "v_mul_f32_e32 v1, v1, %[dinv4]\n\t"
      "v_mul_f32_e32 v2, v2, %[dinv4]\n\t"
      "v_mul_f32_e32 v3, v3, %[dinv4]\n\t"
      "v_cvt_pk_bf16_f32 v32, v0, v1\n\t"
      "v_cvt_pk_bf16_f32 v33, v2, v3\n\t"
      "v_mul_f32_e32 v4, v4, %[dinv4]\n\t"
      "v_mul_f32_e32 v5, v5, %[dinv4]\n\t"
      "v_mul_f32_e32 v6, v6, %[dinv4]\n\t"
      "v_mul_f32_e32 v7, v7, %[dinv4]\n\t"
      "v_cvt_pk_bf16_f32 v34, v4, v5\n\t"
      "v_cvt_pk_bf16_f32 v35, v6, v7\n\t"
      "buffer_store_dwordx4 v[32:35], v92, %[rw], 0 offen sc1\n\t"
      "v_mul_f32_e32 v8, v8, %[dinv4]\n\t"
      "v_mul_f32_e32 v9, v9, %[dinv4]\n\t"
      "v_mul_f32_e32 v10, v10, %[dinv4]\n\t"
      "v_mul_f32_e32 v11, v11, %[dinv4]\n\t"
      "v_cvt_pk_bf16_f32 v36, v8, v9\n\t"
      "v_cvt_pk_bf16_f32 v37, v10, v11\n\t"
      "v_mul_f32_e32 v12, v12, %[dinv4]\n\t"
      "v_mul_f32_e32 v13, v13, %[dinv4]\n\t"
      "v_mul_f32_e32 v14, v14, %[dinv4]\n\t"
      "v_mul_f32_e32 v15, v15, %[dinv4]\n\t"
      "v_cvt_pk_bf16_f32 v38, v12, v13\n\t"
      "v_cvt_pk_bf16_f32 v39, v14, v15\n\t"
      "buffer_store_dwordx4 v[36:39], v92, %[rw], 0 offen offset:1024 sc1\n\t"
      "v_mul_f32_e32 v16, v16, %[dinv4]\n\t"
      "v_mul_f32_e32 v17, v17, %[dinv4]\n\t"
      "v_mul_f32_e32 v18, v18, %[dinv4]\n\t"
      "v_mul_f32_e32 v19, v19, %[dinv4]\n\t"
      "v_cvt_pk_bf16_f32 v40, v16, v17\n\t"
      "v_cvt_pk_bf16_f32 v41, v18, v19\n\t"
      "v_mul_f32_e32 v20, v20, %[dinv4]\n\t"
      "v_mul_f32_e32 v21, v21, %[dinv4]\n\t"
      "v_mul_f32_e32 v22, v22, %[dinv4]\n\t"
      "v_mul_f32_e32 v23, v23, %[dinv4]\n\t"
      "v_cvt_pk_bf16_f32 v42, v20, v21\n\t"
      "v_cvt_pk_bf16_f32 v43, v22, v23\n\t"
      "buffer_store_dwordx4 v[40:43], v92, %[rw], 0 offen offset:2048 sc1\n\t"
      "v_mul_f32_e32 v24, v24, %[dinv4]\n\t"
      "v_mul_f32_e32 v25, v25, %[dinv4]\n\t"
      "v_mul_f32_e32 v26, v26, %[dinv4]\n\t"
      "v_mul_f32_e32 v27, v27, %[dinv4]\n\t"
      "v_cvt_pk_bf16_f32 v44, v24, v25\n\t"
      "v_cvt_pk_bf16_f32 v45, v26, v27\n\t"
      "v_mul_f32_e32 v28, v28, %[dinv4]\n\t"
      "v_mul_f32_e32 v29, v29, %[dinv4]\n\t"
      "v_mul_f32_e32 v30, v30, %[dinv4]\n\t"
      "v_mul_f32_e32 v31, v31, %[dinv4]\n\t"
      "v_cvt_pk_bf16_f32 v46, v28, v29\n\t"
      "v_cvt_pk_bf16_f32 v47, v30, v31\n\t"
      "buffer_store_dwordx4 v[44:47], v92, %[rw], 0 offen offset:3072 sc1\n\t"
      "v_mov_b32_e32 v0, 0\n\t"
      "v_mov_b32_e32 v1, 0\n\t"
      "v_mov_b32_e32 v2, 0\n\t"
      "v_mov_b32_e32 v3, 0\n\t"
      "v_mov_b32_e32 v4, 0\n\t"
      "v_mov_b32_e32 v5, 0\n\t"
      "v_mov_b32_e32 v6, 0\n\t"
      "v_mov_b32_e32 v7, 0\n\t"
      "v_mov_b32_e32 v8, 0\n\t"
      "v_mov_b32_e32 v9, 0\n\t"
      "v_mov_b32_e32 v10, 0\n\t"
      "v_mov_b32_e32 v11, 0\n\t"
      "v_mov_b32_e32 v12, 0\n\t"
      "v_mov_b32_e32 v13, 0\n\t"
      "v_mov_b32_e32 v14, 0\n\t"
      "v_mov_b32_e32 v15, 0\n\t"
      "v_mov_b32_e32 v16, 0\n\t"
      "v_mov_b32_e32 v17, 0\n\t"
      "v_mov_b32_e32 v18, 0\n\t"
      "v_mov_b32_e32 v19, 0\n\t"
      "v_mov_b32_e32 v20, 0\n\t"
      "v_mov_b32_e32 v21, 0\n\t"
      "v_mov_b32_e32 v22, 0\n\t"
      "v_mov_b32_e32 v23, 0\n\t"
      "v_mov_b32_e32 v24, 0\n\t"
      "v_mov_b32_e32 v25, 0\n\t"
      "v_mov_b32_e32 v26, 0\n\t"
      "v_mov_b32_e32 v27, 0\n\t"
      "v_mov_b32_e32 v28, 0\n\t"
      "v_mov_b32_e32 v29, 0\n\t"
      "v_mov_b32_e32 v30, 0\n\t"
      "v_mov_b32_e32 v31, 0\n\t"
      "s_nop 1\n\t"
      "s_waitcnt vmcnt(0)\n\t"
      :
      : [rh] "s"(rh), [rw] "s"(rw), [ringa] "s"(ringa), [so0] "s"(4096u), [so1] "s"(3136u), [so2] "s"(2176u), [so3] "s"(1216u), [msk] "s"(0xffffff00u), [sw] "v"(sw), [vb0] "v"(avb0), [vb1] "v"(avb1), [lpe] "v"(lpe), [lpa] "v"(lpa), [lane16] "v"(lane16), [one_lo] "v"(0x3f80u), [one_hi] "v"(0x3f800000u), [re] "s"(re), [s0] "v"(s0), [s1] "v"(s1), [len0] "v"(len0), [len1] "v"(len1), [lane4] "v"(lane4), [m3c] "s"(0x3cu), [tabv] "v"(tabv), [lst4] "v"(lst4), [nb0] "s"(nb[0]), [st0] "s"(st[0]), [dinv0] "v"(dinv[0]), [key0] "v"(key[0]), [nb1] "s"(nb[1]), [st1] "s"(st[1]), [dinv1] "v"(dinv[1]), [key1] "v"(key[1]), [nb2] "s"(nb[2]), [st2] "s"(st[2]), [dinv2] "v"(dinv[2]), [key2] "v"(key[2]), [nb3] "s"(nb[3]), [st3] "s"(st[3]), [dinv3] "v"(dinv[3]), [key3] "v"(key[3]), [nb4] "s"(nb[4]), [st4] "s"(st[4]), [dinv4] "v"(dinv[4]), [key4] "v"(key[4])
      : "v0", "v1", "v2", "v3", "v4", "v5", "v6", "v7", "v8", "v9", "v10", "v11", "v12", "v13", "v14", "v15", "v16", "v17", "v18", "v19", "v20", "v21", "v22", "v23", "v24", "v25", "v26", "v27", "v28", "v29", "v30", "v31", "v32", "v33", "v34", "v35", "v36", "v37", "v38", "v39", "v40", "v41", "v42", "v43", "v44", "v45", "v46", "v47", "v48", "v49", "v50", "v51", "v52", "v53", "v54", "v55", "v56", "v57", "v58", "v59", "v60", "v61", "v62", "v63", "v64", "v65", "v66", "v67", "v68", "v69", "v70", "v71", "v72", "v73", "v74", "v75", "v76", "v77", "v78", "v79", "v80", "v81", "v82", "v83", "v84", "v85", "v86", "v87", "v88", "v89", "v90", "v91", "v92", "v93", "v94", "v95", "v96", "v97", "v98", "v99", "v100", "v101", "v102", "v103", "v104", "v105", "v106", "v107", "v108", "v109", "v110", "v111", "v112", "v113", "v114", "v115", "v116", "v117", "v118", "v119", "v120", "v121", "v122", "v123", "v124", "v125", "v126", "v127", "v128", "v129", "v130", "v131", "v132", "v133", "v134", "v135", "v136", "v137", "v138", "v139", "v140", "v141", "v142", "v143", "v144", "v145", "v146", "v147", "v148", "v149", "v150", "v151", "v152", "v153", "v154", "v155", "v156", "v157", "v158", "v159", "v160", "v161", "v162", "v163", "v164", "v165", "v166", "v167", "v168", "v169", "v170", "v171", "v172", "v173", "v174", "v175", "v176", "v177", "v178", "v179", "v180", "v181", "v182", "v183", "v184", "v185", "v186", "v187", "v188", "v189", "v190", "v191", "v192", "v193", "v194", "v195", "s60", "s61", "s62", "s63", "s64", "s65", "s66", "s67", "s68", "s69", "s70", "s71", "s84", "s85", "s86", "s87", "s88", "s89", "s90", "s91", "s92", "s93", "s94", "s95", "s96", "s97", "s98", "s99", "m0", "vcc", "scc", "memory");
}

constexpr unsigned OWN_OFF = RING * 16384;
DI void attn_truemax(char* __restrict__ ws, char* lds, unsigned G0, unsigned s, unsigned lane, unsigned w) {
  const unsigned n = lane & 15u, g = lane >> 4, q4 = n >> 2, p = lane & 3u, iv = 4u * g + q4;
  const unsigned kb = n * 64u + 16u * (g ^ xf(n));
  const unsigned vb0 = iv * 64u + 16u * ((p >> 1) ^ xf(iv)) + 8u * (p & 1u), vb1 = vb0 ^ 32u;
#pragma unroll 1
  for (int qt = 0; qt < NQT; ++qt) {
    const unsigned G = G0 + qt;
    bf16x8 qf[4];
#pragma unroll
    for (int ds = 0; ds < 4; ++ds) qf[ds] = asbf8(*(const u32x4*)(ws + OFF_QF + G * 4096u + ds * 1024u + lane * 16u));
    f32x4 o[8];
#pragma unroll
    for (int dt = 0; dt < 8; ++dt) o[dt] = (f32x4){0.f, 0.f, 0.f, 0.f};
    float m = -INFINITY;
#pragma unroll 1
    for (int pass = 0; pass < 2; ++pass) {
      const unsigned k0 = pass ? s * (USTEP * 32u) : 0u, k1 = pass ? k0 + USTEP * 32u : (unsigned)N;
      if (pass) { m = fmaxf(m, __shfl_xor(m, 16)); m = fmaxf(m, __shfl_xor(m, 32)); }
#pragma unroll 1
      for (unsigned t = k0; t < k1; t += 64u) {
        __syncthreads();
#pragma unroll
        for (int k = 0; k < 4; ++k) *(u32x4*)(lds + w * 4096u + k * 1024u + lane * 16u) = *(const u32x4*)(ws + OFF_HROW + (t + 16u * w + (lane >> 2)) * 256u + k * 64u + 16u * ((lane & 3u) ^ xf(lane >> 2)));
        __syncthreads();
        f32x4 sc[4];
#pragma unroll
        for (int kt = 0; kt < 4; ++kt) {
          f32x4 a = {0.f, 0.f, 0.f, 0.f};
#pragma unroll
          for (int ds = 0; ds < 4; ++ds) a = mfma16(asbf8(*(const LDSP(u32x4))(lds + kt * 4096 + ds * 1024 + kb)), qf[ds], a);
#pragma unroll
          for (int r = 0; r < 4; ++r) { const unsigned key = t + 16u * kt + 4u * g + r; if (key >= (unsigned)N || key >= k1) a[r] = -INFINITY; }
          sc[kt] = a;
        }
        if (pass == 0) {
#pragma unroll
          for (int kt = 0; kt < 4; ++kt) m = fmaxf(m, fmaxf(fmaxf(sc[kt][0], sc[kt][1]), fmaxf(sc[kt][2], sc[kt][3])));
        } else {
#pragma unroll
          for (int km = 0; km < 2; ++km) {
            float pe[8];
#pragma unroll
            for (int j = 0; j < 8; ++j) pe[j] = __builtin_amdgcn_exp2f(sc[2 * km + (j >> 2)][j & 3] - m);
            u32x4 pw = {pk2(pe[0], pe[1]), pk2(pe[2], pe[3]), pk2(pe[4], pe[5]), pk2(pe[6], pe[7])};
#pragma unroll
            for (int dt = 0; dt < 8; ++dt) {
              const char* a = lds + (2 * km) * 4096 + (dt >> 1) * 1024 + ((dt & 1) ? vb1 : vb0);
              o[dt] = mfma16(cat8(trread(a), trread(a + 4096)), asbf8(pw), o[dt]);
            }
          }
        }
      }
    }
#pragma unroll
    for (int s4 = 0; s4 < 4; ++s4) {
      u32x4 a = {pk2(o[2 * s4][0], o[2 * s4][1]), pk2(o[2 * s4][2], o[2 * s4][3]), pk2(o[2 * s4 + 1][0], o[2 * s4 + 1][1]), pk2(o[2 * s4 + 1][2], o[2 * s4 + 1][3])};
      const unsigned gi = (G % (4u * NQT)) - K3G * s;
      if (gi < (unsigned)K3G) *(u32x4*)(lds + OWN_OFF + gi * 4096u + s4 * 1024u + lane * 16u) = a;
      else bst16d(mk_rsrc(ws, WS_USED), (G * NSPLIT + s) * 4096u + s4 * 1024u + lane * 16u, OFF_PO, a);
    }
  }
}

constexpr int K2LDS = RING * 16384 > 4 * (int)SPW ? RING * 16384 : 4 * (int)SPW;
DI void final_stage(const uint16_t* __restrict__ x, char* __restrict__ ws, uint16_t* __restrict__ out, char* lds, unsigned b, unsigned s) {
  const unsigned tid = threadIdx.x, lane = tid & 63u, w = rfl(tid >> 6), n = lane & 15u, g = lane >> 4;
  const unsigned Gb = 4u * NQT * b + K3G * s, ng = s == NSPLIT - 1 ? 4 * NQT - K3G * (NSPLIT - 1) : K3G;
  const rsrc_t rw = mk_rsrc(ws, WS_USED);
  unsigned Gc[K3G];
#pragma unroll
  for (int gi = 0; gi < K3G; ++gi) Gc[gi] = Gb + gi < (unsigned)NG && gi < (int)ng ? Gb + gi : (Gb < (unsigned)NG ? Gb : (unsigned)(NG - 1));
  u32x4 po[K3G][NSPLIT], a2[K3G]; u32x2 xv[K3G][2];
  bf16x8 wf[2][2][4];
#pragma unroll
  for (int gi = 0; gi < K3G; ++gi)
#pragma unroll
    for (int fh = 0; fh < 2; ++fh) xv[gi][fh] = *(const u32x2*)(x + (16u * Gc[gi] + n) * D + 16u * (2u * w + fh) + 4u * g);
  wg_wait2<true>(ws, NQB + b, 1u, 2u * NQB, 1u);
  wg_arrive_stores(ws, b);
  STAMP(ws, 4096u + blockIdx.x * 4u + w, 1);
#pragma unroll
  for (int m = 0; m < 2; ++m)
#pragma unroll
    for (int fh = 0; fh < 2; ++fh)
#pragma unroll
      for (int k = 0; k < 4; ++k) wf[m][fh][k] = asbf8(bld16d(rw, lane * 16u, OFF_WF + m * 32768u + ((2u * w + fh) * 4u + k) * 1024u));
#pragma unroll
  for (int gi = 0; gi < K3G; ++gi) a2[gi] = bld16d(rw, lane * 16u, OFF_AGG + Gc[gi] * 4096u + w * 1024u);
  SB();
  wg_wait2<false>(ws, b, NSPLIT, b, NSPLIT);
  STAMP(ws, 4096u + blockIdx.x * 4u + w, 3);
#pragma unroll
  for (int gi = 0; gi < K3G; ++gi) {
#pragma unroll
    for (int k = 0; k < NSPLIT; ++k) po[gi][k] = bld16d(rw, (unsigned)k == s ? OOB : lane * 16u, OFF_PO + (Gc[gi] * NSPLIT + k) * 4096u + w * 1024u);
    SB();
  }
  bf16x8 sa, sb; make_selectors(lane, sa, sb);
  float* lsum = (float*)(lds + K3G * 2 * 4096);
#pragma unroll
  for (int gi = 0; gi < K3G; ++gi) {
    f32x4 oa = {0.f, 0.f, 0.f, 0.f}, ob = oa;
    { const bf16x8 mine = asbf8(*(const u32x4*)(lds + OWN_OFF + gi * 4096 + w * 1024u + lane * 16u)); oa = mfma16(sa, mine, oa); ob = mfma16(sb, mine, ob); }
#pragma unroll
    for (int k = 0; k < NSPLIT; ++k) { oa = mfma16(sa, asbf8(po[gi][k]), oa); ob = mfma16(sb, asbf8(po[gi][k]), ob); }
    if (gi == 0) STAMP(ws, 4096u + blockIdx.x * 4u + w, 4);
    if (gi == K3G - 1) STAMP(ws, 4096u + blockIdx.x * 4u + w, 5);
    if (w == 3u && g == 3u) lsum[gi * 16 + n] = ob[3];
    *(u32x4*)(lds + (gi * 2 + 1) * 4096 + w * 1024u + lane * 16u) = (u32x4){pk2(oa[0], oa[1]), pk2(oa[2], oa[3]), pk2(ob[0], ob[1]), pk2(ob[2], ob[3])};
    *(u32x4*)(lds + (gi * 2 + 0) * 4096 + w * 1024u + lane * 16u) = a2[gi];
    asm volatile("s_waitcnt lgkmcnt(0)\n\ts_barrier" ::: "memory");
    f32x4 y[2][2];
#pragma unroll
    for (int m = 0; m < 2; ++m) {
      y[m][0] = (f32x4){0.f, 0.f, 0.f, 0.f}; y[m][1] = y[m][0];
#pragma unroll
      for (int k = 0; k < 4; ++k) {
        const bf16x8 bfr = asbf8(*(const u32x4*)(lds + (gi * 2 + m) * 4096 + k * 1024 + lane * 16u));
        y[m][0] = mfma16(wf[m][0][k], bfr, y[m][0]); y[m][1] = mfma16(wf[m][1][k], bfr, y[m][1]);
      }
    }
    const float il = 1.0f / lsum[gi * 16 + n];
    if (Gb + gi < (unsigned)NG && gi < (int)ng) {
#pragma unroll
      for (int fh = 0; fh < 2; ++fh) {
        const f32x4 yy = y[0][fh] + y[1][fh] * il;
        u32x2 o = {pk2(bflo(xv[gi][fh][0]) + yy[0], bfhi(xv[gi][fh][0]) + yy[1]), pk2(bflo(xv[gi][fh][1]) + yy[2], bfhi(xv[gi][fh][1]) + yy[3])};
        *(u32x2*)(out + (16u * Gc[gi] + n) * D + 16u * (2u * w + fh) + 4u * g) = o;
      }
    }
    SB();
  }
}

__global__ __launch_bounds__(256, 1) void k2_main(const uint16_t* __restrict__ w_msg, const uint16_t* __restrict__ wv, const uint16_t* __restrict__ w_out,
                                                  const uint16_t* __restrict__ deg_inv, const uint16_t* __restrict__ x, char* __restrict__ ws, uint16_t* __restrict__ out) {
  extern __shared__ __attribute__((aligned(16))) char lds[];
  CODE_PC(pc2, "L_gps_k2");
  const unsigned tid = threadIdx.x, lane = tid & 63u, wid = rfl(tid >> 6), bid = blockIdx.x;
  const unsigned xc = bid & 7u, k = bid >> 3;
  if (__builtin_expect(k >= (unsigned)(NATT / 8), 0)) {
    const unsigned j = (k - NATT / 8) * 8u + xc;
    STAMP(ws, 2048u + bid * 4u + wid, 0);
    XCC(ws, 2048u + bid * 4u + wid);
    char* L = lds + wid * SPW;
    STAMP(ws, 2048u + bid * 4u + wid, 1);
    const unsigned wv_id = j * 4u + wid;
    if (wv_id * TPW < (unsigned)NG) agg_wave(ws, deg_inv, L, wv_id, lane);
    else if (wv_id >= NSPARE * 4u - 2u) weight_prep(wv_id & 1u ? wv : w_msg, w_out, ws, L, (int)(wv_id & 1u), lane);
    STAMP(ws, 2048u + bid * 4u + wid, 6);
    wg_arrive(ws, NQB + j);
    if (j == NSPARE - 1 && tid == 0) __hip_atomic_fetch_add((gu32*)(ws + OFF_CNT + 2u * NQB * 256u), 1u, __ATOMIC_RELAXED, __HIP_MEMORY_SCOPE_AGENT);
    CODE_MARK("L_gps_k2f");
    return;
  }
  {
    const unsigned b = xc + 8u * (k / NSPLIT), s = k % NSPLIT;
    const unsigned G0 = 4u * NQT * b + NQT * wid;
    const unsigned foff = rfl(OFF_FLAG + 4u * NQT * b * 4u), flane = (lane < 4u * NQT ? lane : 0u) * 4u;
    unsigned slow;
    unsigned own[NQT];
#pragma unroll
    for (int qt = 0; qt < NQT; ++qt) { const unsigned gi = NQT * wid + qt - K3G * s; own[qt] = rfl(gi < (unsigned)K3G ? (unsigned)(size_t)lds + OWN_OFF + gi * 4096u : 0u); }
    const unsigned ldsb = (unsigned)(size_t)lds;
    const unsigned i16 = lane & 15u, g = lane >> 4;
    const unsigned kb = ldsb + 64u * i16 + 16u * (g ^ xf(i16));
    const unsigned q4 = i16 >> 2, p = lane & 3u, iv = 4u * g + q4;
    const unsigned vb0 = ldsb + 64u * iv + 16u * ((p >> 1) ^ xf(iv)) + 8u * (p & 1u), vb1 = vb0 ^ 32u;
    const unsigned lane16 = lane * 16u;
    CODE_OFF(c2e, "L_gps_k2e", "L_gps_k2");
    const unsigned cofs = (wid * 64u + lane) * 128u, coff = cofs < c2e ? cofs : 0u;
    __amdgpu_buffer_rsrc_t srd = __builtin_amdgcn_make_buffer_rsrc((void*)ws, (short)0, (int)WS_USED, 0x00020000);
    const rsrc_t hsrd = mk_rsrc(ws + OFF_HROW - 4096, 10240u * 256u + 4096u);
    const unsigned dvb = (lane >> 2) * 256u + 16u * ((lane & 3u) ^ xf(lane >> 2)) + 4096u;
    const unsigned dv0 = dvb, dv1 = dvb - 960u, dv2 = dvb - 1920u, dv3 = dvb - 2880u;
    const unsigned hoff = rfl(s * (USTEP * 8192u) + 4096u * wid);
    const unsigned woff = rfl((k * 4u + wid) * 24576u + 4096u), wlane = lane * 128u;
    const unsigned qoff = rfl(OFF_QF + G0 * 4096u);
    const unsigned pooff = rfl(OFF_PO + (G0 * NSPLIT + s) * 4096u);
    const unsigned wlds = rfl(ldsb + 4096u * wid);
    const unsigned stoff = rfl(OFF_ST + (bid * 4u + wid) * 64u);
    asm volatile(
    "s_nop 4\n\t"
    "s_mov_b32 s61, %[hoff]\n\t"
    "s_mov_b32 %[slow], 0\n\t"
    "buffer_load_dword v223, %[flane], %[srd], %[foff] offen\n\t"
    "s_add_u32 m0, %[wlds], 0\n\t"
    "s_nop 0\n\t"
    "buffer_load_dwordx4 %[dv0], %[hsrd], s61 offen lds\n\t"
    "buffer_load_dwordx4 %[dv1], %[hsrd], s61 offen offset:1024 lds\n\t"
    "buffer_load_dwordx4 %[dv2], %[hsrd], s61 offen offset:2048 lds\n\t"
    "buffer_load_dwordx4 %[dv3], %[hsrd], s61 offen offset:3072 lds\n\t"
    "global_load_dword v224, %[coff], %[pc2]\n\t"
    "s_add_u32 s61, s61, 0x4000\n\t"
    "s_add_u32 s66, %[qoff], 0\n\t"
    "buffer_load_dwordx4 a[0:3], %[lane16], %[srd], s66 offen\n\t"
    "v_mov_b32_e32 v120, 0\n\t"
    "v_mov_b32_e32 v121, 0\n\t"
    "v_mov_b32_e32 v122, 0\n\t"
    "v_mov_b32_e32 v123, 0\n\t"
    "v_mov_b32_e32 v124, 0\n\t"
    "v_mov_b32_e32 v125, 0\n\t"
    "buffer_load_dwordx4 a[4:7], %[lane16], %[srd], s66 offen offset:1024\n\t"
    "v_mov_b32_e32 v126, 0\n\t"
    "v_mov_b32_e32 v127, 0\n\t"
    "v_mov_b32_e32 v128, 0\n\t"
    "v_mov_b32_e32 v129, 0\n\t"
    "v_mov_b32_e32 v130, 0\n\t"
    "v_mov_b32_e32 v131, 0\n\t"
    "buffer_load_dwordx4 a[8:11], %[lane16], %[srd], s66 offen offset:2048\n\t"
    "v_mov_b32_e32 v132, 0\n\t"
    "v_mov_b32_e32 v133, 0\n\t"
    "v_mov_b32_e32 v134, 0\n\t"
    "v_mov_b32_e32 v135, 0\n\t"
    "v_mov_b32_e32 v136, 0\n\t"
    "v_mov_b32_e32 v137, 0\n\t"
    "buffer_load_dwordx4 a[12:15], %[lane16], %[srd], s66 offen offset:3072\n\t"
    "v_mov_b32_e32 v138, 0\n\t"
    "v_mov_b32_e32 v139, 0\n\t"
    "v_mov_b32_e32 v140, 0\n\t"
    "v_mov_b32_e32 v141, 0\n\t"
    "v_mov_b32_e32 v142, 0\n\t"
    "v_mov_b32_e32 v143, 0\n\t"
    "s_add_u32 s66, %[qoff], 4096\n\t"
    "buffer_load_dwordx4 a[16:19], %[lane16], %[srd], s66 offen\n\t"
    "v_mov_b32_e32 v144, 0\n\t"
    "v_mov_b32_e32 v145, 0\n\t"
    "v_mov_b32_e32 v146, 0\n\t"
    "v_mov_b32_e32 v147, 0\n\t"
    "v_mov_b32_e32 v148, 0\n\t"
    "v_mov_b32_e32 v149, 0\n\t"
    "buffer_load_dwordx4 a[20:23], %[lane16], %[srd], s66 offen offset:1024\n\t"
    "v_mov_b32_e32 v150, 0\n\t"
    "v_mov_b32_e32 v151, 0\n\t"
    "v_mov_b32_e32 v152, 0\n\t"
    "v_mov_b32_e32 v153, 0\n\t"
    "v_mov_b32_e32 v154, 0\n\t"
    "v_mov_b32_e32 v155, 0\n\t"
    "buffer_load_dwordx4 a[24:27], %[lane16], %[srd], s66 offen offset:2048\n\t"
    "v_mov_b32_e32 v156, 0\n\t"
    "v_mov_b32_e32 v157, 0\n\t"
    "v_mov_b32_e32 v158, 0\n\t"
    "v_mov_b32_e32 v159, 0\n\t"
    "v_mov_b32_e32 v160, 0\n\t"
    "v_mov_b32_e32 v161, 0\n\t"
    "buffer_load_dwordx4 a[28:31], %[lane16], %[srd], s66 offen offset:3072\n\t"
    "v_mov_b32_e32 v162, 0\n\t"
    "v_mov_b32_e32 v163, 0\n\t"
    "v_mov_b32_e32 v164, 0\n\t"
    "v_mov_b32_e32 v165, 0\n\t"
    "v_mov_b32_e32 v166, 0\n\t"
    "v_mov_b32_e32 v167, 0\n\t"
    "s_add_u32 s66, %[qoff], 8192\n\t"
    "buffer_load_dwordx4 a[32:35], %[lane16], %[srd], s66 offen\n\t"
    "v_mov_b32_e32 v168, 0\n\t"
    "v_mov_b32_e32 v169, 0\n\t"
    "v_mov_b32_e32 v170, 0\n\t"
    "v_mov_b32_e32 v171, 0\n\t"
    "v_mov_b32_e32 v172, 0\n\t"
    "v_mov_b32_e32 v173, 0\n\t"
    "buffer_load_dwordx4 a[36:39], %[lane16], %[srd], s66 offen offset:1024\n\t"
    "v_mov_b32_e32 v174, 0\n\t"
    "v_mov_b32_e32 v175, 0\n\t"
    "v_mov_b32_e32 v176, 0\n\t"
    "v_mov_b32_e32 v177, 0\n\t"
    "v_mov_b32_e32 v178, 0\n\t"
    "v_mov_b32_e32 v179, 0\n\t"
    "buffer_load_dwordx4 a[40:43], %[lane16], %[srd], s66 offen offset:2048\n\t"
    "v_mov_b32_e32 v180, 0\n\t"
    "v_mov_b32_e32 v181, 0\n\t"
    "v_mov_b32_e32 v182, 0\n\t"
    "v_mov_b32_e32 v183, 0\n\t"
    "v_mov_b32_e32 v184, 0\n\t"
    "v_mov_b32_e32 v185, 0\n\t"
    "buffer_load_dwordx4 a[44:47], %[lane16], %[srd], s66 offen offset:3072\n\t"
    "v_mov_b32_e32 v186, 0\n\t"
    "v_mov_b32_e32 v187, 0\n\t"
    "v_mov_b32_e32 v188, 0\n\t"
    "v_mov_b32_e32 v189, 0\n\t"
    "v_mov_b32_e32 v190, 0\n\t"
    "v_mov_b32_e32 v191, 0\n\t"
    "s_add_u32 s66, %[qoff], 12288\n\t"
    "buffer_load_dwordx4 a[48:51], %[lane16], %[srd], s66 offen\n\t"
    "v_mov_b32_e32 v192, 0\n\t"
    "v_mov_b32_e32 v193, 0\n\t"
    "v_mov_b32_e32 v194, 0\n\t"
    "v_mov_b32_e32 v195, 0\n\t"
    "v_mov_b32_e32 v196, 0\n\t"
    "v_mov_b32_e32 v197, 0\n\t"
    "buffer_load_dwordx4 a[52:55], %[lane16], %[srd], s66 offen offset:1024\n\t"
    "v_mov_b32_e32 v198, 0\n\t"
    "v_mov_b32_e32 v199, 0\n\t"
    "v_mov_b32_e32 v200, 0\n\t"
    "v_mov_b32_e32 v201, 0\n\t"
    "v_mov_b32_e32 v202, 0\n\t"
    "v_mov_b32_e32 v203, 0\n\t"
    "buffer_load_dwordx4 a[56:59], %[lane16], %[srd], s66 offen offset:2048\n\t"
    "v_mov_b32_e32 v204, 0\n\t"
    "v_mov_b32_e32 v205, 0\n\t"
    "v_mov_b32_e32 v206, 0\n\t"
    "v_mov_b32_e32 v207, 0\n\t"
    "v_mov_b32_e32 v208, 0\n\t"
    "v_mov_b32_e32 v209, 0\n\t"
    "buffer_load_dwordx4 a[60:63], %[lane16], %[srd], s66 offen offset:3072\n\t"
    "v_mov_b32_e32 v210, 0\n\t"
    "v_mov_b32_e32 v211, 0\n\t"
    "v_mov_b32_e32 v212, 0\n\t"
    "v_mov_b32_e32 v213, 0\n\t"
    "v_mov_b32_e32 v214, 0\n\t"
    "v_mov_b32_e32 v215, 0\n\t"
    "s_add_u32 s66, %[qoff], 16384\n\t"
    "buffer_load_dwordx4 a[64:67], %[lane16], %[srd], s66 offen\n\t"
    "v_mov_b32_e32 v216, 0\n\t"
    "v_mov_b32_e32 v217, 0\n\t"
    "v_mov_b32_e32 v218, 0\n\t"
    "v_mov_b32_e32 v219, 0\n\t"
    "v_accvgpr_write_b32 a144, 0\n\t"
    "v_accvgpr_write_b32 a145, 0\n\t"
    "buffer_load_dwordx4 a[68:71], %[lane16], %[srd], s66 offen offset:1024\n\t"
    "v_accvgpr_write_b32 a146, 0\n\t"
    "v_accvgpr_write_b32 a147, 0\n\t"
    "v_accvgpr_write_b32 a148, 0\n\t"
    "v_accvgpr_write_b32 a149, 0\n\t"
    "v_accvgpr_write_b32 a150, 0\n\t"
    "v_accvgpr_write_b32 a151, 0\n\t"
    "buffer_load_dwordx4 a[72:75], %[lane16], %[srd], s66 offen offset:2048\n\t"
    "v_accvgpr_write_b32 a152, 0\n\t"
    "v_accvgpr_write_b32 a153, 0\n\t"
    "v_accvgpr_write_b32 a154, 0\n\t"
    "v_accvgpr_write_b32 a155, 0\n\t"
    "v_accvgpr_write_b32 a156, 0\n\t"
    "v_accvgpr_write_b32 a157, 0\n\t"
    "buffer_load_dwordx4 a[76:79], %[lane16], %[srd], s66 offen offset:3072\n\t"
    "v_accvgpr_write_b32 a158, 0\n\t"
    "v_accvgpr_write_b32 a159, 0\n\t"
    "v_accvgpr_write_b32 a160, 0\n\t"
    "v_accvgpr_write_b32 a161, 0\n\t"
    "v_accvgpr_write_b32 a162, 0\n\t"
    "v_accvgpr_write_b32 a163, 0\n\t"
    "s_add_u32 m0, %[wlds], 16384\n\t"
    "s_nop 0\n\t"
    "buffer_load_dwordx4 %[dv0], %[hsrd], s61 offen lds\n\t"
    "buffer_load_dwordx4 %[dv1], %[hsrd], s61 offen offset:1024 lds\n\t"
    "buffer_load_dwordx4 %[dv2], %[hsrd], s61 offen offset:2048 lds\n\t"
    "buffer_load_dwordx4 %[dv3], %[hsrd], s61 offen offset:3072 lds\n\t"
    "v_accvgpr_write_b32 a164, 0\n\t"
    "v_accvgpr_write_b32 a165, 0\n\t"
    "v_accvgpr_write_b32 a166, 0\n\t"
    "v_accvgpr_write_b32 a167, 0\n\t"
    "v_accvgpr_write_b32 a168, 0\n\t"
    "v_accvgpr_write_b32 a169, 0\n\t"
    "v_accvgpr_write_b32 a170, 0\n\t"
    "v_accvgpr_write_b32 a171, 0\n\t"
    "v_accvgpr_write_b32 a172, 0\n\t"
    "v_accvgpr_write_b32 a173, 0\n\t"
    "v_accvgpr_write_b32 a174, 0\n\t"
    "v_accvgpr_write_b32 a175, 0\n\t"
    "v_accvgpr_write_b32 a176, 0\n\t"
    "v_accvgpr_write_b32 a177, 0\n\t"
    "v_accvgpr_write_b32 a178, 0\n\t"
    "v_accvgpr_write_b32 a179, 0\n\t"
    "v_accvgpr_write_b32 a180, 0\n\t"
    "v_accvgpr_write_b32 a181, 0\n\t"
    "v_accvgpr_write_b32 a182, 0\n\t"
    "v_accvgpr_write_b32 a183, 0\n\t"
    "v_accvgpr_write_b32 a184, 0\n\t"
    "v_accvgpr_write_b32 a185, 0\n\t"
    "v_accvgpr_write_b32 a186, 0\n\t"
    "v_accvgpr_write_b32 a187, 0\n\t"
    "v_accvgpr_write_b32 a188, 0\n\t"
    "v_accvgpr_write_b32 a189, 0\n\t"
    "v_accvgpr_write_b32 a190, 0\n\t"
    "v_accvgpr_write_b32 a191, 0\n\t"
    "v_accvgpr_write_b32 a192, 0\n\t"
    "v_accvgpr_write_b32 a193, 0\n\t"
    "v_accvgpr_write_b32 a194, 0\n\t"
    "v_accvgpr_write_b32 a195, 0\n\t"
    "v_accvgpr_write_b32 a196, 0\n\t"
    "v_accvgpr_write_b32 a197, 0\n\t"
    "v_accvgpr_write_b32 a198, 0\n\t"
    "v_accvgpr_write_b32 a199, 0\n\t"
    "v_accvgpr_write_b32 a200, 0\n\t"
    "v_accvgpr_write_b32 a201, 0\n\t"
    "v_accvgpr_write_b32 a202, 0\n\t"
    "v_accvgpr_write_b32 a203, 0\n\t"
    "v_mov_b32_e32 v220, %[kb]\n\t"
    "v_mov_b32_e32 v221, %[vb0]\n\t"
    "v_mov_b32_e32 v222, %[vb1]\n\t"
    "s_mov_b32 s62, 98304\n\t"
    "s_mov_b32 s63, 0x4000\n\t"
    "s_mov_b32 s64, 0\n\t"
    "s_mov_b32 s60, 20\n\t"
    "s_waitcnt vmcnt(25)\n\t"
    "v_cmp_ne_u32_e32 vcc, 0, v223\n\t"
    "s_barrier\n\t"
    "s_cbranch_vccnz L_attn_slow_%=\n\t"
    "ds_read_b128 a[80:83], v220 offset:0\n\t"
    "ds_read_b128 a[84:87], v220 offset:1024\n\t"
    "ds_read_b128 a[88:91], v220 offset:2048\n\t"
    "ds_read_b128 a[92:95], v220 offset:3072\n\t"
    "ds_read_b128 a[96:99], v220 offset:4096\n\t"
    "ds_read_b128 a[100:103], v220 offset:5120\n\t"
    "ds_read_b128 a[104:107], v220 offset:6144\n\t"
    "ds_read_b128 a[108:111], v220 offset:7168\n\t"
    "s_waitcnt lgkmcnt(0)\n\t"
    "s_waitcnt vmcnt(20)\n\t"
    "v_mfma_f32_16x16x32_bf16 v[0:3], a[80:83], a[0:3], 0\n\t"
    "v_mfma_f32_16x16x32_bf16 v[0:3], a[84:87], a[4:7], v[0:3]\n\t"
    "v_mfma_f32_16x16x32_bf16 v[0:3], a[88:91], a[8:11], v[0:3]\n\t"
    "v_mfma_f32_16x16x32_bf16 v[0:3], a[92:95], a[12:15], v[0:3]\n\t"
    "s_waitcnt vmcnt(16)\n\t"
    "v_mfma_f32_16x16x32_bf16 v[4:7], a[80:83], a[16:19], 0\n\t"
    "v_mfma_f32_16x16x32_bf16 v[4:7], a[84:87], a[20:23], v[4:7]\n\t"
    "v_mfma_f32_16x16x32_bf16 v[4:7], a[88:91], a[24:27], v[4:7]\n\t"
    "v_mfma_f32_16x16x32_bf16 v[4:7], a[92:95], a[28:31], v[4:7]\n\t"
    "s_waitcnt vmcnt(12)\n\t"
    "v_mfma_f32_16x16x32_bf16 v[8:11], a[80:83], a[32:35], 0\n\t"
    "v_mfma_f32_16x16x32_bf16 v[8:11], a[84:87], a[36:39], v[8:11]\n\t"
    "v_mfma_f32_16x16x32_bf16 v[8:11], a[88:91], a[40:43], v[8:11]\n\t"
    "v_mfma_f32_16x16x32_bf16 v[8:11], a[92:95], a[44:47], v[8:11]\n\t"
    "s_waitcnt vmcnt(8)\n\t"
    "v_mfma_f32_16x16x32_bf16 v[12:15], a[80:83], a[48:51], 0\n\t"
    "v_mfma_f32_16x16x32_bf16 v[12:15], a[84:87], a[52:55], v[12:15]\n\t"
    "v_mfma_f32_16x16x32_bf16 v[12:15], a[88:91], a[56:59], v[12:15]\n\t"
    "v_mfma_f32_16x16x32_bf16 v[12:15], a[92:95], a[60:63], v[12:15]\n\t"
    "s_waitcnt vmcnt(4)\n\t"
    "v_mfma_f32_16x16x32_bf16 v[16:19], a[80:83], a[64:67], 0\n\t"
    "ds_read_b128 a[80:83], v220 offset:8192\n\t"
    "v_mfma_f32_16x16x32_bf16 v[16:19], a[84:87], a[68:71], v[16:19]\n\t"
    "ds_read_b128 a[84:87], v220 offset:9216\n\t"
    "v_mfma_f32_16x16x32_bf16 v[16:19], a[88:91], a[72:75], v[16:19]\n\t"
    "ds_read_b128 a[88:91], v220 offset:10240\n\t"
    "v_mfma_f32_16x16x32_bf16 v[16:19], a[92:95], a[76:79], v[16:19]\n\t"
    "ds_read_b128 a[92:95], v220 offset:11264\n\t"
    "v_mfma_f32_16x16x32_bf16 v[20:23], a[96:99], a[0:3], 0\n\t"
    "s_add_u32 s61, s61, 0x4000\n\t"
    "s_add_u32 m0, %[wlds], 32768\n\t"
    "s_nop 0\n\t"
    "buffer_load_dwordx4 %[dv0], %[hsrd], s61 offen lds\n\t"
    "v_mfma_f32_16x16x32_bf16 v[20:23], a[100:103], a[4:7], v[20:23]\n\t"
    "v_mfma_f32_16x16x32_bf16 v[20:23], a[104:107], a[8:11], v[20:23]\n\t"
    "buffer_load_dwordx4 %[dv1], %[hsrd], s61 offen offset:1024 lds\n\t"
    "v_mfma_f32_16x16x32_bf16 v[20:23], a[108:111], a[12:15], v[20:23]\n\t"
    "v_mfma_f32_16x16x32_bf16 v[24:27], a[96:99], a[16:19], 0\n\t"
    "buffer_load_dwordx4 %[dv2], %[hsrd], s61 offen offset:2048 lds\n\t"
    "v_mfma_f32_16x16x32_bf16 v[24:27], a[100:103], a[20:23], v[24:27]\n\t"
    "v_mfma_f32_16x16x32_bf16 v[24:27], a[104:107], a[24:27], v[24:27]\n\t"
    "buffer_load_dwordx4 %[dv3], %[hsrd], s61 offen offset:3072 lds\n\t"
    "v_mfma_f32_16x16x32_bf16 v[24:27], a[108:111], a[28:31], v[24:27]\n\t"
    "v_mfma_f32_16x16x32_bf16 v[28:31], a[96:99], a[32:35], 0\n\t"
    "s_add_u32 s61, s61, 0x4000\n\t"
    "s_add_u32 m0, %[wlds], 49152\n\t"
    "s_nop 0\n\t"
    "buffer_load_dwordx4 %[dv0], %[hsrd], s61 offen lds\n\t"
    "v_mfma_f32_16x16x32_bf16 v[28:31], a[100:103], a[36:39], v[28:31]\n\t"
    "v_mfma_f32_16x16x32_bf16 v[28:31], a[104:107], a[40:43], v[28:31]\n\t"
    "buffer_load_dwordx4 %[dv1], %[hsrd], s61 offen offset:1024 lds\n\t"
    "v_mfma_f32_16x16x32_bf16 v[28:31], a[108:111], a[44:47], v[28:31]\n\t"
    "v_mfma_f32_16x16x32_bf16 v[32:35], a[96:99], a[48:51], 0\n\t"
    "buffer_load_dwordx4 %[dv2], %[hsrd], s61 offen offset:2048 lds\n\t"
    "v_mfma_f32_16x16x32_bf16 v[32:35], a[100:103], a[52:55], v[32:35]\n\t"
    "v_mfma_f32_16x16x32_bf16 v[32:35], a[104:107], a[56:59], v[32:35]\n\t"
    "buffer_load_dwordx4 %[dv3], %[hsrd], s61 offen offset:3072 lds\n\t"
    "v_mfma_f32_16x16x32_bf16 v[32:35], a[108:111], a[60:63], v[32:35]\n\t"
    "v_mfma_f32_16x16x32_bf16 v[36:39], a[96:99], a[64:67], 0\n\t"
    "ds_read_b128 a[96:99], v220 offset:12288\n\t"
    "v_mfma_f32_16x16x32_bf16 v[36:39], a[100:103], a[68:71], v[36:39]\n\t"
    "ds_read_b128 a[100:103], v220 offset:13312\n\t"
    "v_mfma_f32_16x16x32_bf16 v[36:39], a[104:107], a[72:75], v[36:39]\n\t"
    "ds_read_b128 a[104:107], v220 offset:14336\n\t"
    "v_mfma_f32_16x16x32_bf16 v[36:39], a[108:111], a[76:79], v[36:39]\n\t"
    "ds_read_b128 a[108:111], v220 offset:15360\n\t"
    "v_add_u32_e32 v220, s63, %[kb]\n\t"
    "s_waitcnt lgkmcnt(0)\n\t"
    "v_mfma_f32_16x16x32_bf16 v[40:43], a[80:83], a[0:3], 0\n\t"
    "v_exp_f32_e32 v0, v0\n\t"
    "v_mfma_f32_16x16x32_bf16 v[40:43], a[84:87], a[4:7], v[40:43]\n\t"
    "v_mfma_f32_16x16x32_bf16 v[40:43], a[88:91], a[8:11], v[40:43]\n\t"
    "v_exp_f32_e32 v1, v1\n\t"
    "v_mfma_f32_16x16x32_bf16 v[40:43], a[92:95], a[12:15], v[40:43]\n\t"
    "s_add_u32 s61, s61, 0x4000\n\t"
    "s_add_u32 m0, %[wlds], 65536\n\t"
    "s_nop 0\n\t"
    "buffer_load_dwordx4 %[dv0], %[hsrd], s61 offen lds\n\t"
    "v_mfma_f32_16x16x32_bf16 v[44:47], a[80:83], a[16:19], 0\n\t"
    "v_exp_f32_e32 v2, v2\n\t"
    "v_mfma_f32_16x16x32_bf16 v[44:47], a[84:87], a[20:23], v[44:47]\n\t"
    "v_cvt_pk_bf16_f32 v80, v0, v1\n\t"
    "v_mfma_f32_16x16x32_bf16 v[44:47], a[88:91], a[24:27], v[44:47]\n\t"
    "v_exp_f32_e32 v3, v3\n\t"
    "v_mfma_f32_16x16x32_bf16 v[44:47], a[92:95], a[28:31], v[44:47]\n\t"
    "buffer_load_dwordx4 %[dv1], %[hsrd], s61 offen offset:1024 lds\n\t"
    "v_mfma_f32_16x16x32_bf16 v[48:51], a[80:83], a[32:35], 0\n\t"
    "v_exp_f32_e32 v4, v4\n\t"
    "v_mfma_f32_16x16x32_bf16 v[48:51], a[84:87], a[36:39], v[48:51]\n\t"
    "v_cvt_pk_bf16_f32 v81, v2, v3\n\t"
    "v_mfma_f32_16x16x32_bf16 v[48:51], a[88:91], a[40:43], v[48:51]\n\t"
    "v_exp_f32_e32 v5, v5\n\t"
    "v_mfma_f32_16x16x32_bf16 v[48:51], a[92:95], a[44:47], v[48:51]\n\t"
    "buffer_load_dwordx4 %[dv2], %[hsrd], s61 offen offset:2048 lds\n\t"
    "v_mfma_f32_16x16x32_bf16 v[52:55], a[80:83], a[48:51], 0\n\t"
    "v_exp_f32_e32 v6, v6\n\t"
    "v_mfma_f32_16x16x32_bf16 v[52:55], a[84:87], a[52:55], v[52:55]\n\t"
    "v_cvt_pk_bf16_f32 v84, v4, v5\n\t"
    "v_mfma_f32_16x16x32_bf16 v[52:55], a[88:91], a[56:59], v[52:55]\n\t"
    "v_exp_f32_e32 v7, v7\n\t"
    "v_mfma_f32_16x16x32_bf16 v[52:55], a[92:95], a[60:63], v[52:55]\n\t"
    "buffer_load_dwordx4 %[dv3], %[hsrd], s61 offen offset:3072 lds\n\t"
    "v_mfma_f32_16x16x32_bf16 v[56:59], a[80:83], a[64:67], 0\n\t"
    "v_exp_f32_e32 v8, v8\n\t"
    "v_mfma_f32_16x16x32_bf16 v[56:59], a[84:87], a[68:71], v[56:59]\n\t"
    "v_cvt_pk_bf16_f32 v85, v6, v7\n\t"
    "v_mfma_f32_16x16x32_bf16 v[56:59], a[88:91], a[72:75], v[56:59]\n\t"
    "v_exp_f32_e32 v9, v9\n\t"
    "v_mfma_f32_16x16x32_bf16 v[56:59], a[92:95], a[76:79], v[56:59]\n\t"
    "s_add_u32 s61, s61, 0x4000\n\t"
    "s_add_u32 m0, %[wlds], 81920\n\t"
    "s_nop 0\n\t"
    "buffer_load_dwordx4 %[dv0], %[hsrd], s61 offen lds\n\t"
    "v_mfma_f32_16x16x32_bf16 v[60:63], a[96:99], a[0:3], 0\n\t"
    "v_exp_f32_e32 v10, v10\n\t"
    "v_mfma_f32_16x16x32_bf16 v[60:63], a[100:103], a[4:7], v[60:63]\n\t"
    "v_cvt_pk_bf16_f32 v88, v8, v9\n\t"
    "v_mfma_f32_16x16x32_bf16 v[60:63], a[104:107], a[8:11], v[60:63]\n\t"
    "v_exp_f32_e32 v11, v11\n\t"
    "v_mfma_f32_16x16x32_bf16 v[60:63], a[108:111], a[12:15], v[60:63]\n\t"
    "buffer_load_dwordx4 %[dv1], %[hsrd], s61 offen offset:1024 lds\n\t"
    "v_mfma_f32_16x16x32_bf16 v[64:67], a[96:99], a[16:19], 0\n\t"
    "v_exp_f32_e32 v12, v12\n\t"
    "v_mfma_f32_16x16x32_bf16 v[64:67], a[100:103], a[20:23], v[64:67]\n\t"
    "v_cvt_pk_bf16_f32 v89, v10, v11\n\t"
    "v_mfma_f32_16x16x32_bf16 v[64:67], a[104:107], a[24:27], v[64:67]\n\t"
    "v_exp_f32_e32 v13, v13\n\t"
    "v_mfma_f32_16x16x32_bf16 v[64:67], a[108:111], a[28:31], v[64:67]\n\t"
    "buffer_load_dwordx4 %[dv2], %[hsrd], s61 offen offset:2048 lds\n\t"
    "v_mfma_f32_16x16x32_bf16 v[68:71], a[96:99], a[32:35], 0\n\t"
    "v_exp_f32_e32 v14, v14\n\t"
    "v_mfma_f32_16x16x32_bf16 v[68:71], a[100:103], a[36:39], v[68:71]\n\t"
    "v_cvt_pk_bf16_f32 v92, v12, v13\n\t"
    "v_mfma_f32_16x16x32_bf16 v[68:71], a[104:107], a[40:43], v[68:71]\n\t"
    "v_exp_f32_e32 v15, v15\n\t"
    "v_mfma_f32_16x16x32_bf16 v[68:71], a[108:111], a[44:47], v[68:71]\n\t"
    "buffer_load_dwordx4 %[dv3], %[hsrd], s61 offen offset:3072 lds\n\t"
    "v_mfma_f32_16x16x32_bf16 v[72:75], a[96:99], a[48:51], 0\n\t"
    "v_exp_f32_e32 v16, v16\n\t"
    "v_mfma_f32_16x16x32_bf16 v[72:75], a[100:103], a[52:55], v[72:75]\n\t"
    "v_cvt_pk_bf16_f32 v93, v14, v15\n\t"
    "v_mfma_f32_16x16x32_bf16 v[72:75], a[104:107], a[56:59], v[72:75]\n\t"
    "v_exp_f32_e32 v17, v17\n\t"
    "v_mfma_f32_16x16x32_bf16 v[72:75], a[108:111], a[60:63], v[72:75]\n\t"
    "s_waitcnt vmcnt(16)\n\t"
    "s_add_u32 s61, s61, 0x4000\n\t"
    "s_add_u32 m0, %[wlds], 98304\n\t"
    "s_nop 0\n\t"
    "buffer_load_dwordx4 %[dv0], %[hsrd], s61 offen lds\n\t"
    "v_mfma_f32_16x16x32_bf16 v[76:79], a[96:99], a[64:67], 0\n\t"
    "v_exp_f32_e32 v18, v18\n\t"
    "v_mfma_f32_16x16x32_bf16 v[76:79], a[100:103], a[68:71], v[76:79]\n\t"
    "v_cvt_pk_bf16_f32 v96, v16, v17\n\t"
    "v_mfma_f32_16x16x32_bf16 v[76:79], a[104:107], a[72:75], v[76:79]\n\t"
    "v_exp_f32_e32 v19, v19\n\t"
    "v_mfma_f32_16x16x32_bf16 v[76:79], a[108:111], a[76:79], v[76:79]\n\t"
    "s_barrier\n\t"
    "buffer_load_dwordx4 %[dv1], %[hsrd], s61 offen offset:1024 lds\n\t"
    "v_exp_f32_e32 v20, v20\n\t"
    "ds_read_b128 a[80:83], v220 offset:0\n\t"
    "v_cvt_pk_bf16_f32 v97, v18, v19\n\t"
    "v_exp_f32_e32 v21, v21\n\t"
    "ds_read_b128 a[84:87], v220 offset:1024\n\t"
    "s_add_u32 s66, %[woff], 0\n\t"
    "buffer_load_dword v224, %[wlane], %[hsrd], s66 offen\n\t"
    "v_exp_f32_e32 v22, v22\n\t"
    "ds_read_b128 a[88:91], v220 offset:2048\n\t"
    "v_cvt_pk_bf16_f32 v82, v20, v21\n\t"
    "v_exp_f32_e32 v23, v23\n\t"
    "ds_read_b128 a[92:95], v220 offset:3072\n\t"
    "s_add_u32 s66, %[woff], 8192\n\t"
    "buffer_load_dword v225, %[wlane], %[hsrd], s66 offen\n\t"
    "v_exp_f32_e32 v24, v24\n\t"
    "ds_read_b128 a[96:99], v220 offset:4096\n\t"
    "v_cvt_pk_bf16_f32 v83, v22, v23\n\t"
    "v_exp_f32_e32 v25, v25\n\t"
    "ds_read_b128 a[100:103], v220 offset:5120\n\t"
    "s_add_u32 s66, %[woff], 16384\n\t"
    "buffer_load_dword v226, %[wlane], %[hsrd], s66 offen\n\t"
    "v_exp_f32_e32 v26, v26\n\t"
    "ds_read_b128 a[104:107], v220 offset:6144\n\t"
    "v_cvt_pk_bf16_f32 v86, v24, v25\n\t"
    "v_exp_f32_e32 v27, v27\n\t"
    "ds_read_b128 a[108:111], v220 offset:7168\n\t"
    "v_exp_f32_e32 v28, v28\n\t"
    "v_cvt_pk_bf16_f32 v87, v26, v27\n\t"
    "v_exp_f32_e32 v29, v29\n\t"
    "v_exp_f32_e32 v30, v30\n\t"
    "v_cvt_pk_bf16_f32 v90, v28, v29\n\t"
    "v_exp_f32_e32 v31, v31\n\t"
    "v_exp_f32_e32 v32, v32\n\t"
    "v_cvt_pk_bf16_f32 v91, v30, v31\n\t"
    "v_exp_f32_e32 v33, v33\n\t"
    "v_exp_f32_e32 v34, v34\n\t"
    "v_cvt_pk_bf16_f32 v94, v32, v33\n\t"
    "v_exp_f32_e32 v35, v35\n\t"
    "v_exp_f32_e32 v36, v36\n\t"
    "s_waitcnt lgkmcnt(0)\n\t"
    "v_cvt_pk_bf16_f32 v95, v34, v35\n\t"
    "v_exp_f32_e32 v37, v37\n\t"
    "v_exp_f32_e32 v38, v38\n\t"
    "v_cvt_pk_bf16_f32 v98, v36, v37\n\t"
    "v_exp_f32_e32 v39, v39\n\t"
    ".p2align 6\n\t"
    "L_attn_loop_%=:\n\t"
    "v_mfma_f32_16x16x32_bf16 v[0:3], a[80:83], a[0:3], 0\n\t"
    "v_exp_f32_e32 v40, v40\n\t"
    "v_mfma_f32_16x16x32_bf16 v[0:3], a[84:87], a[4:7], v[0:3]\n\t"
    "ds_read_b64_tr_b16 a[112:113], v221 offset:0\n\t"
    "v_cvt_pk_bf16_f32 v99, v38, v39\n\t"
    "v_mfma_f32_16x16x32_bf16 v[0:3], a[88:91], a[8:11], v[0:3]\n\t"
    "v_exp_f32_e32 v41, v41\n\t"
    "v_mfma_f32_16x16x32_bf16 v[0:3], a[92:95], a[12:15], v[0:3]\n\t"
    "ds_read_b64_tr_b16 a[114:115], v221 offset:4096\n\t"
    "s_add_u32 s64, s63, 0\n\t"
    "v_mfma_f32_16x16x32_bf16 v[4:7], a[80:83], a[16:19], 0\n\t"
    "v_exp_f32_e32 v42, v42\n\t"
    "v_mfma_f32_16x16x32_bf16 v[4:7], a[84:87], a[20:23], v[4:7]\n\t"
    "ds_read_b64_tr_b16 a[116:117], v222 offset:0\n\t"
    "v_cvt_pk_bf16_f32 v100, v40, v41\n\t"
    "v_mfma_f32_16x16x32_bf16 v[4:7], a[88:91], a[24:27], v[4:7]\n\t"
    "v_exp_f32_e32 v43, v43\n\t"
    "v_mfma_f32_16x16x32_bf16 v[4:7], a[92:95], a[28:31], v[4:7]\n\t"
    "ds_read_b64_tr_b16 a[118:119], v222 offset:4096\n\t"
    "s_add_u32 s63, s63, 0x4000\n\t"
    "v_mfma_f32_16x16x32_bf16 v[8:11], a[80:83], a[32:35], 0\n\t"
    "v_exp_f32_e32 v44, v44\n\t"
    "v_mfma_f32_16x16x32_bf16 v[8:11], a[84:87], a[36:39], v[8:11]\n\t"
    "ds_read_b64_tr_b16 a[120:121], v221 offset:1024\n\t"
    "v_cvt_pk_bf16_f32 v101, v42, v43\n\t"
    "v_mfma_f32_16x16x32_bf16 v[8:11], a[88:91], a[40:43], v[8:11]\n\t"
    "v_exp_f32_e32 v45, v45\n\t"
    "v_mfma_f32_16x16x32_bf16 v[8:11], a[92:95], a[44:47], v[8:11]\n\t"
    "ds_read_b64_tr_b16 a[122:123], v221 offset:5120\n\t"
    "s_and_b32 s63, s63, 0x1ffff\n\t"
    "v_mfma_f32_16x16x32_bf16 v[12:15], a[80:83], a[48:51], 0\n\t"
    "v_exp_f32_e32 v46, v46\n\t"
    "v_mfma_f32_16x16x32_bf16 v[12:15], a[84:87], a[52:55], v[12:15]\n\t"
    "ds_read_b64_tr_b16 a[124:125], v222 offset:1024\n\t"
    "v_cvt_pk_bf16_f32 v104, v44, v45\n\t"
    "v_mfma_f32_16x16x32_bf16 v[12:15], a[88:91], a[56:59], v[12:15]\n\t"
    "v_exp_f32_e32 v47, v47\n\t"
    "v_mfma_f32_16x16x32_bf16 v[12:15], a[92:95], a[60:63], v[12:15]\n\t"
    "ds_read_b64_tr_b16 a[126:127], v222 offset:5120\n\t"
    "s_add_u32 s62, s62, 0x4000\n\t"
    "v_mfma_f32_16x16x32_bf16 v[16:19], a[80:83], a[64:67], 0\n\t"
    "v_exp_f32_e32 v48, v48\n\t"
    "v_mfma_f32_16x16x32_bf16 v[16:19], a[84:87], a[68:71], v[16:19]\n\t"
    "ds_read_b64_tr_b16 a[128:129], v221 offset:2048\n\t"
    "v_cvt_pk_bf16_f32 v105, v46, v47\n\t"
    "v_mfma_f32_16x16x32_bf16 v[16:19], a[88:91], a[72:75], v[16:19]\n\t"
    "v_exp_f32_e32 v49, v49\n\t"
    "v_mfma_f32_16x16x32_bf16 v[16:19], a[92:95], a[76:79], v[16:19]\n\t"
    "ds_read_b64_tr_b16 a[130:131], v221 offset:6144\n\t"
    "s_and_b32 s62, s62, 0x1ffff\n\t"
    "v_mfma_f32_16x16x32_bf16 v[20:23], a[96:99], a[0:3], 0\n\t"
    "v_exp_f32_e32 v50, v50\n\t"
    "v_mfma_f32_16x16x32_bf16 v[20:23], a[100:103], a[4:7], v[20:23]\n\t"
    "ds_read_b64_tr_b16 a[132:133], v222 offset:2048\n\t"
    "v_cvt_pk_bf16_f32 v108, v48, v49\n\t"
    "v_mfma_f32_16x16x32_bf16 v[20:23], a[104:107], a[8:11], v[20:23]\n\t"
    "v_exp_f32_e32 v51, v51\n\t"
    "v_mfma_f32_16x16x32_bf16 v[20:23], a[108:111], a[12:15], v[20:23]\n\t"
    "ds_read_b64_tr_b16 a[134:135], v222 offset:6144\n\t"
    "s_waitcnt lgkmcnt(4)\n\t"
    "v_mfma_f32_16x16x32_bf16 v[24:27], a[96:99], a[16:19], 0\n\t"
    "v_exp_f32_e32 v52, v52\n\t"
    "v_mfma_f32_16x16x32_bf16 v[24:27], a[100:103], a[20:23], v[24:27]\n\t"
    "ds_read_b64_tr_b16 a[136:137], v221 offset:3072\n\t"
    "v_cvt_pk_bf16_f32 v109, v50, v51\n\t"
    "v_mfma_f32_16x16x32_bf16 v[24:27], a[104:107], a[24:27], v[24:27]\n\t"
    "v_exp_f32_e32 v53, v53\n\t"
    "v_mfma_f32_16x16x32_bf16 v[24:27], a[108:111], a[28:31], v[24:27]\n\t"
    "ds_read_b64_tr_b16 a[138:139], v221 offset:7168\n\t"
    "v_mfma_f32_16x16x32_bf16 v[28:31], a[96:99], a[32:35], 0\n\t"
    "v_exp_f32_e32 v54, v54\n\t"
    "v_mfma_f32_16x16x32_bf16 v[28:31], a[100:103], a[36:39], v[28:31]\n\t"
    "ds_read_b64_tr_b16 a[140:141], v222 offset:3072\n\t"
    "v_cvt_pk_bf16_f32 v112, v52, v53\n\t"
    "v_mfma_f32_16x16x32_bf16 v[28:31], a[104:107], a[40:43], v[28:31]\n\t"
    "v_exp_f32_e32 v55, v55\n\t"
    "v_mfma_f32_16x16x32_bf16 v[28:31], a[108:111], a[44:47], v[28:31]\n\t"
    "ds_read_b64_tr_b16 a[142:143], v222 offset:7168\n\t"
    "v_mfma_f32_16x16x32_bf16 v[32:35], a[96:99], a[48:51], 0\n\t"
    "v_exp_f32_e32 v56, v56\n\t"
    "v_mfma_f32_16x16x32_bf16 v[32:35], a[100:103], a[52:55], v[32:35]\n\t"
    "v_cvt_pk_bf16_f32 v113, v54, v55\n\t"
    "v_mfma_f32_16x16x32_bf16 v[32:35], a[104:107], a[56:59], v[32:35]\n\t"
    "v_exp_f32_e32 v57, v57\n\t"
    "v_mfma_f32_16x16x32_bf16 v[32:35], a[108:111], a[60:63], v[32:35]\n\t"
    "v_mfma_f32_16x16x32_bf16 v[36:39], a[96:99], a[64:67], 0\n\t"
    "v_exp_f32_e32 v58, v58\n\t"
    "v_mfma_f32_16x16x32_bf16 v[36:39], a[100:103], a[68:71], v[36:39]\n\t"
    "v_cvt_pk_bf16_f32 v116, v56, v57\n\t"
    "v_mfma_f32_16x16x32_bf16 v[36:39], a[104:107], a[72:75], v[36:39]\n\t"
    "v_exp_f32_e32 v59, v59\n\t"
    "v_mfma_f32_16x16x32_bf16 v[36:39], a[108:111], a[76:79], v[36:39]\n\t"
    "v_mfma_f32_16x16x32_bf16 v[120:123], a[112:115], v[80:83], v[120:123]\n\t"
    "v_exp_f32_e32 v60, v60\n\t"
    "v_mfma_f32_16x16x32_bf16 v[124:127], a[112:115], v[84:87], v[124:127]\n\t"
    "ds_read_b128 a[80:83], v220 offset:8192\n\t"
    "v_cvt_pk_bf16_f32 v117, v58, v59\n\t"
    "v_mfma_f32_16x16x32_bf16 v[128:131], a[112:115], v[88:91], v[128:131]\n\t"
    "v_exp_f32_e32 v61, v61\n\t"
    "v_mfma_f32_16x16x32_bf16 v[132:135], a[112:115], v[92:95], v[132:135]\n\t"
    "ds_read_b128 a[84:87], v220 offset:9216\n\t"
    "v_mfma_f32_16x16x32_bf16 v[136:139], a[112:115], v[96:99], v[136:139]\n\t"
    "v_exp_f32_e32 v62, v62\n\t"
    "v_mfma_f32_16x16x32_bf16 v[140:143], a[116:119], v[80:83], v[140:143]\n\t"
    "ds_read_b128 a[88:91], v220 offset:10240\n\t"
    "v_cvt_pk_bf16_f32 v102, v60, v61\n\t"
    "v_mfma_f32_16x16x32_bf16 v[144:147], a[116:119], v[84:87], v[144:147]\n\t"
    "v_exp_f32_e32 v63, v63\n\t"
    "v_mfma_f32_16x16x32_bf16 v[148:151], a[116:119], v[88:91], v[148:151]\n\t"
    "ds_read_b128 a[92:95], v220 offset:11264\n\t"
    "v_mfma_f32_16x16x32_bf16 v[152:155], a[116:119], v[92:95], v[152:155]\n\t"
    "v_exp_f32_e32 v64, v64\n\t"
    "v_mfma_f32_16x16x32_bf16 v[156:159], a[116:119], v[96:99], v[156:159]\n\t"
    "ds_read_b128 a[96:99], v220 offset:12288\n\t"
    "v_cvt_pk_bf16_f32 v103, v62, v63\n\t"
    "v_mfma_f32_16x16x32_bf16 v[160:163], a[120:123], v[80:83], v[160:163]\n\t"
    "v_exp_f32_e32 v65, v65\n\t"
    "v_mfma_f32_16x16x32_bf16 v[164:167], a[120:123], v[84:87], v[164:167]\n\t"
    "ds_read_b128 a[100:103], v220 offset:13312\n\t"
    "s_waitcnt lgkmcnt(6)\n\t"
    "v_mfma_f32_16x16x32_bf16 v[168:171], a[120:123], v[88:91], v[168:171]\n\t"
    "v_exp_f32_e32 v66, v66\n\t"
    "v_mfma_f32_16x16x32_bf16 v[172:175], a[120:123], v[92:95], v[172:175]\n\t"
    "ds_read_b128 a[104:107], v220 offset:14336\n\t"
    "v_cvt_pk_bf16_f32 v106, v64, v65\n\t"
    "v_mfma_f32_16x16x32_bf16 v[176:179], a[120:123], v[96:99], v[176:179]\n\t"
    "v_exp_f32_e32 v67, v67\n\t"
    "v_mfma_f32_16x16x32_bf16 v[180:183], a[124:127], v[80:83], v[180:183]\n\t"
    "ds_read_b128 a[108:111], v220 offset:15360\n\t"
    "v_mfma_f32_16x16x32_bf16 v[184:187], a[124:127], v[84:87], v[184:187]\n\t"
    "v_exp_f32_e32 v68, v68\n\t"
    "v_mfma_f32_16x16x32_bf16 v[188:191], a[124:127], v[88:91], v[188:191]\n\t"
    "v_cvt_pk_bf16_f32 v107, v66, v67\n\t"
    "v_mfma_f32_16x16x32_bf16 v[192:195], a[124:127], v[92:95], v[192:195]\n\t"
    "v_exp_f32_e32 v69, v69\n\t"
    "v_mfma_f32_16x16x32_bf16 v[196:199], a[124:127], v[96:99], v[196:199]\n\t"
    "v_mfma_f32_16x16x32_bf16 v[200:203], a[128:131], v[80:83], v[200:203]\n\t"
    "v_exp_f32_e32 v70, v70\n\t"
    "v_mfma_f32_16x16x32_bf16 v[204:207], a[128:131], v[84:87], v[204:207]\n\t"
    "v_cvt_pk_bf16_f32 v110, v68, v69\n\t"
    "v_mfma_f32_16x16x32_bf16 v[208:211], a[128:131], v[88:91], v[208:211]\n\t"
    "v_exp_f32_e32 v71, v71\n\t"
    "v_mfma_f32_16x16x32_bf16 v[212:215], a[128:131], v[92:95], v[212:215]\n\t"
    "v_mfma_f32_16x16x32_bf16 v[216:219], a[128:131], v[96:99], v[216:219]\n\t"
    "v_exp_f32_e32 v72, v72\n\t"
    "v_mfma_f32_16x16x32_bf16 a[144:147], a[132:135], v[80:83], a[144:147]\n\t"
    "v_cvt_pk_bf16_f32 v111, v70, v71\n\t"
    "v_mfma_f32_16x16x32_bf16 a[148:151], a[132:135], v[84:87], a[148:151]\n\t"
    "v_exp_f32_e32 v73, v73\n\t"
    "v_mfma_f32_16x16x32_bf16 a[152:155], a[132:135], v[88:91], a[152:155]\n\t"
    "buffer_load_dwordx4 %[dv2], %[hsrd], s61 offen offset:2048 lds\n\t"
    "v_mfma_f32_16x16x32_bf16 a[156:159], a[132:135], v[92:95], a[156:159]\n\t"
    "v_exp_f32_e32 v74, v74\n\t"
    "v_mfma_f32_16x16x32_bf16 a[160:163], a[132:135], v[96:99], a[160:163]\n\t"
    "v_cvt_pk_bf16_f32 v114, v72, v73\n\t"
    "v_mfma_f32_16x16x32_bf16 a[164:167], a[136:139], v[80:83], a[164:167]\n\t"
    "v_exp_f32_e32 v75, v75\n\t"
    "v_mfma_f32_16x16x32_bf16 a[168:171], a[136:139], v[84:87], a[168:171]\n\t"
    "v_mfma_f32_16x16x32_bf16 a[172:175], a[136:139], v[88:91], a[172:175]\n\t"
    "v_exp_f32_e32 v76, v76\n\t"
    "v_mfma_f32_16x16x32_bf16 a[176:179], a[136:139], v[92:95], a[176:179]\n\t"
    "s_waitcnt lgkmcnt(0)\n\t"
    "v_cvt_pk_bf16_f32 v115, v74, v75\n\t"
    "v_mfma_f32_16x16x32_bf16 a[180:183], a[136:139], v[96:99], a[180:183]\n\t"
    "v_exp_f32_e32 v77, v77\n\t"
    "v_mfma_f32_16x16x32_bf16 a[184:187], a[140:143], v[80:83], a[184:187]\n\t"
    "buffer_load_dwordx4 %[dv3], %[hsrd], s61 offen offset:3072 lds\n\t"
    "v_mfma_f32_16x16x32_bf16 a[188:191], a[140:143], v[84:87], a[188:191]\n\t"
    "v_exp_f32_e32 v78, v78\n\t"
    "v_mfma_f32_16x16x32_bf16 a[192:195], a[140:143], v[88:91], a[192:195]\n\t"
    "v_cvt_pk_bf16_f32 v118, v76, v77\n\t"
    "v_mfma_f32_16x16x32_bf16 a[196:199], a[140:143], v[92:95], a[196:199]\n\t"
    "v_exp_f32_e32 v79, v79\n\t"
    "v_mfma_f32_16x16x32_bf16 a[200:203], a[140:143], v[96:99], a[200:203]\n\t"
    "v_mfma_f32_16x16x32_bf16 v[40:43], a[80:83], a[0:3], 0\n\t"
    "v_exp_f32_e32 v0, v0\n\t"
    "v_mfma_f32_16x16x32_bf16 v[40:43], a[84:87], a[4:7], v[40:43]\n\t"
    "ds_read_b64_tr_b16 a[112:113], v221 offset:8192\n\t"
    "v_cvt_pk_bf16_f32 v119, v78, v79\n\t"
    "v_mfma_f32_16x16x32_bf16 v[40:43], a[88:91], a[8:11], v[40:43]\n\t"
    "v_exp_f32_e32 v1, v1\n\t"
    "v_mfma_f32_16x16x32_bf16 v[40:43], a[92:95], a[12:15], v[40:43]\n\t"
    "ds_read_b64_tr_b16 a[114:115], v221 offset:12288\n\t"
    "s_add_u32 s61, s61, 0x4000\n\t"
    "v_mfma_f32_16x16x32_bf16 v[44:47], a[80:83], a[16:19], 0\n\t"
    "v_exp_f32_e32 v2, v2\n\t"
    "v_mfma_f32_16x16x32_bf16 v[44:47], a[84:87], a[20:23], v[44:47]\n\t"
    "ds_read_b64_tr_b16 a[116:117], v222 offset:8192\n\t"
    "v_cvt_pk_bf16_f32 v80, v0, v1\n\t"
    "v_mfma_f32_16x16x32_bf16 v[44:47], a[88:91], a[24:27], v[44:47]\n\t"
    "v_exp_f32_e32 v3, v3\n\t"
    "v_mfma_f32_16x16x32_bf16 v[44:47], a[92:95], a[28:31], v[44:47]\n\t"
    "ds_read_b64_tr_b16 a[118:119], v222 offset:12288\n\t"
    "v_add_u32_e32 v220, s63, %[kb]\n\t"
    "v_mfma_f32_16x16x32_bf16 v[48:51], a[80:83], a[32:35], 0\n\t"
    "v_exp_f32_e32 v4, v4\n\t"
    "v_mfma_f32_16x16x32_bf16 v[48:51], a[84:87], a[36:39], v[48:51]\n\t"
    "ds_read_b64_tr_b16 a[120:121], v221 offset:9216\n\t"
    "v_cvt_pk_bf16_f32 v81, v2, v3\n\t"
    "v_mfma_f32_16x16x32_bf16 v[48:51], a[88:91], a[40:43], v[48:51]\n\t"
    "v_exp_f32_e32 v5, v5\n\t"
    "v_mfma_f32_16x16x32_bf16 v[48:51], a[92:95], a[44:47], v[48:51]\n\t"
    "ds_read_b64_tr_b16 a[122:123], v221 offset:13312\n\t"
    "s_add_u32 m0, s62, %[wlds]\n\t"
    "v_mfma_f32_16x16x32_bf16 v[52:55], a[80:83], a[48:51], 0\n\t"
    "v_exp_f32_e32 v6, v6\n\t"
    "v_mfma_f32_16x16x32_bf16 v[52:55], a[84:87], a[52:55], v[52:55]\n\t"
    "ds_read_b64_tr_b16 a[124:125], v222 offset:9216\n\t"
    "v_cvt_pk_bf16_f32 v84, v4, v5\n\t"
    "v_mfma_f32_16x16x32_bf16 v[52:55], a[88:91], a[56:59], v[52:55]\n\t"
    "v_exp_f32_e32 v7, v7\n\t"
    "v_mfma_f32_16x16x32_bf16 v[52:55], a[92:95], a[60:63], v[52:55]\n\t"
    "ds_read_b64_tr_b16 a[126:127], v222 offset:13312\n\t"
    "v_mfma_f32_16x16x32_bf16 v[56:59], a[80:83], a[64:67], 0\n\t"
    "v_exp_f32_e32 v8, v8\n\t"
    "v_mfma_f32_16x16x32_bf16 v[56:59], a[84:87], a[68:71], v[56:59]\n\t"
    "ds_read_b64_tr_b16 a[128:129], v221 offset:10240\n\t"
    "v_cvt_pk_bf16_f32 v85, v6, v7\n\t"
    "v_mfma_f32_16x16x32_bf16 v[56:59], a[88:91], a[72:75], v[56:59]\n\t"
    "v_exp_f32_e32 v9, v9\n\t"
    "v_mfma_f32_16x16x32_bf16 v[56:59], a[92:95], a[76:79], v[56:59]\n\t"
    "ds_read_b64_tr_b16 a[130:131], v221 offset:14336\n\t"
    "v_mfma_f32_16x16x32_bf16 v[60:63], a[96:99], a[0:3], 0\n\t"
    "v_exp_f32_e32 v10, v10\n\t"
    "v_mfma_f32_16x16x32_bf16 v[60:63], a[100:103], a[4:7], v[60:63]\n\t"
    "ds_read_b64_tr_b16 a[132:133], v222 offset:10240\n\t"
    "v_cvt_pk_bf16_f32 v88, v8, v9\n\t"
    "v_mfma_f32_16x16x32_bf16 v[60:63], a[104:107], a[8:11], v[60:63]\n\t"
    "v_exp_f32_e32 v11, v11\n\t"
    "v_mfma_f32_16x16x32_bf16 v[60:63], a[108:111], a[12:15], v[60:63]\n\t"
    "ds_read_b64_tr_b16 a[134:135], v222 offset:14336\n\t"
    "s_waitcnt lgkmcnt(4)\n\t"
    "v_mfma_f32_16x16x32_bf16 v[64:67], a[96:99], a[16:19], 0\n\t"
    "v_exp_f32_e32 v12, v12\n\t"
    "v_mfma_f32_16x16x32_bf16 v[64:67], a[100:103], a[20:23], v[64:67]\n\t"
    "ds_read_b64_tr_b16 a[136:137], v221 offset:11264\n\t"
    "v_cvt_pk_bf16_f32 v89, v10, v11\n\t"
    "v_mfma_f32_16x16x32_bf16 v[64:67], a[104:107], a[24:27], v[64:67]\n\t"
    "v_exp_f32_e32 v13, v13\n\t"
    "v_mfma_f32_16x16x32_bf16 v[64:67], a[108:111], a[28:31], v[64:67]\n\t"
    "ds_read_b64_tr_b16 a[138:139], v221 offset:15360\n\t"
    "v_mfma_f32_16x16x32_bf16 v[68:71], a[96:99], a[32:35], 0\n\t"
    "v_exp_f32_e32 v14, v14\n\t"
    "v_mfma_f32_16x16x32_bf16 v[68:71], a[100:103], a[36:39], v[68:71]\n\t"
    "ds_read_b64_tr_b16 a[140:141], v222 offset:11264\n\t"
    "v_cvt_pk_bf16_f32 v92, v12, v13\n\t"
    "v_mfma_f32_16x16x32_bf16 v[68:71], a[104:107], a[40:43], v[68:71]\n\t"
    "v_exp_f32_e32 v15, v15\n\t"
    "v_mfma_f32_16x16x32_bf16 v[68:71], a[108:111], a[44:47], v[68:71]\n\t"
    "ds_read_b64_tr_b16 a[142:143], v222 offset:15360\n\t"
    "v_mfma_f32_16x16x32_bf16 v[72:75], a[96:99], a[48:51], 0\n\t"
    "v_exp_f32_e32 v16, v16\n\t"
    "v_mfma_f32_16x16x32_bf16 v[72:75], a[100:103], a[52:55], v[72:75]\n\t"
    "v_cvt_pk_bf16_f32 v93, v14, v15\n\t"
    "v_mfma_f32_16x16x32_bf16 v[72:75], a[104:107], a[56:59], v[72:75]\n\t"
    "v_exp_f32_e32 v17, v17\n\t"
    "v_mfma_f32_16x16x32_bf16 v[72:75], a[108:111], a[60:63], v[72:75]\n\t"
    "s_waitcnt vmcnt(16)\n\t"
    "v_mfma_f32_16x16x32_bf16 v[76:79], a[96:99], a[64:67], 0\n\t"
    "v_exp_f32_e32 v18, v18\n\t"
    "v_mfma_f32_16x16x32_bf16 v[76:79], a[100:103], a[68:71], v[76:79]\n\t"
    "v_cvt_pk_bf16_f32 v96, v16, v17\n\t"
    "v_mfma_f32_16x16x32_bf16 v[76:79], a[104:107], a[72:75], v[76:79]\n\t"
    "v_exp_f32_e32 v19, v19\n\t"
    "v_mfma_f32_16x16x32_bf16 v[76:79], a[108:111], a[76:79], v[76:79]\n\t"
    "s_barrier\n\t"
    "v_mfma_f32_16x16x32_bf16 v[120:123], a[112:115], v[100:103], v[120:123]\n\t"
    "v_exp_f32_e32 v20, v20\n\t"
    "v_mfma_f32_16x16x32_bf16 v[124:127], a[112:115], v[104:107], v[124:127]\n\t"
    "ds_read_b128 a[80:83], v220 offset:0\n\t"
    "v_cvt_pk_bf16_f32 v97, v18, v19\n\t"
    "v_mfma_f32_16x16x32_bf16 v[128:131], a[112:115], v[108:111], v[128:131]\n\t"
    "v_exp_f32_e32 v21, v21\n\t"
    "v_mfma_f32_16x16x32_bf16 v[132:135], a[112:115], v[112:115], v[132:135]\n\t"
    "ds_read_b128 a[84:87], v220 offset:1024\n\t"
    "v_mfma_f32_16x16x32_bf16 v[136:139], a[112:115], v[116:119], v[136:139]\n\t"
    "v_exp_f32_e32 v22, v22\n\t"
    "v_mfma_f32_16x16x32_bf16 v[140:143], a[116:119], v[100:103], v[140:143]\n\t"
    "ds_read_b128 a[88:91], v220 offset:2048\n\t"
    "v_cvt_pk_bf16_f32 v82, v20, v21\n\t"
    "v_mfma_f32_16x16x32_bf16 v[144:147], a[116:119], v[104:107], v[144:147]\n\t"
    "v_exp_f32_e32 v23, v23\n\t"
    "v_mfma_f32_16x16x32_bf16 v[148:151], a[116:119], v[108:111], v[148:151]\n\t"
    "ds_read_b128 a[92:95], v220 offset:3072\n\t"
    "v_mfma_f32_16x16x32_bf16 v[152:155], a[116:119], v[112:115], v[152:155]\n\t"
    "v_exp_f32_e32 v24, v24\n\t"
    "v_mfma_f32_16x16x32_bf16 v[156:159], a[116:119], v[116:119], v[156:159]\n\t"
    "ds_read_b128 a[96:99], v220 offset:4096\n\t"
    "v_cvt_pk_bf16_f32 v83, v22, v23\n\t"
    "v_mfma_f32_16x16x32_bf16 v[160:163], a[120:123], v[100:103], v[160:163]\n\t"
    "v_exp_f32_e32 v25, v25\n\t"
    "v_mfma_f32_16x16x32_bf16 v[164:167], a[120:123], v[104:107], v[164:167]\n\t"
    "ds_read_b128 a[100:103], v220 offset:5120\n\t"
    "s_waitcnt lgkmcnt(6)\n\t"
    "v_mfma_f32_16x16x32_bf16 v[168:171], a[120:123], v[108:111], v[168:171]\n\t"
    "v_exp_f32_e32 v26, v26\n\t"
    "v_mfma_f32_16x16x32_bf16 v[172:175], a[120:123], v[112:115], v[172:175]\n\t"
    "ds_read_b128 a[104:107], v220 offset:6144\n\t"
    "v_cvt_pk_bf16_f32 v86, v24, v25\n\t"
    "v_mfma_f32_16x16x32_bf16 v[176:179], a[120:123], v[116:119], v[176:179]\n\t"
    "v_exp_f32_e32 v27, v27\n\t"
    "v_mfma_f32_16x16x32_bf16 v[180:183], a[124:127], v[100:103], v[180:183]\n\t"
    "ds_read_b128 a[108:111], v220 offset:7168\n\t"
    "v_mfma_f32_16x16x32_bf16 v[184:187], a[124:127], v[104:107], v[184:187]\n\t"
    "v_exp_f32_e32 v28, v28\n\t"
    "v_mfma_f32_16x16x32_bf16 v[188:191], a[124:127], v[108:111], v[188:191]\n\t"
    "v_cvt_pk_bf16_f32 v87, v26, v27\n\t"
    "v_mfma_f32_16x16x32_bf16 v[192:195], a[124:127], v[112:115], v[192:195]\n\t"
    "v_exp_f32_e32 v29, v29\n\t"
    "v_mfma_f32_16x16x32_bf16 v[196:199], a[124:127], v[116:119], v[196:199]\n\t"
    "v_mfma_f32_16x16x32_bf16 v[200:203], a[128:131], v[100:103], v[200:203]\n\t"
    "v_exp_f32_e32 v30, v30\n\t"
    "v_mfma_f32_16x16x32_bf16 v[204:207], a[128:131], v[104:107], v[204:207]\n\t"
    "v_cvt_pk_bf16_f32 v90, v28, v29\n\t"
    "v_mfma_f32_16x16x32_bf16 v[208:211], a[128:131], v[108:111], v[208:211]\n\t"
    "v_exp_f32_e32 v31, v31\n\t"
    "v_mfma_f32_16x16x32_bf16 v[212:215], a[128:131], v[112:115], v[212:215]\n\t"
    "v_mfma_f32_16x16x32_bf16 v[216:219], a[128:131], v[116:119], v[216:219]\n\t"
    "v_exp_f32_e32 v32, v32\n\t"
    "v_mfma_f32_16x16x32_bf16 a[144:147], a[132:135], v[100:103], a[144:147]\n\t"
    "v_cvt_pk_bf16_f32 v91, v30, v31\n\t"
    "v_mfma_f32_16x16x32_bf16 a[148:151], a[132:135], v[104:107], a[148:151]\n\t"
    "v_exp_f32_e32 v33, v33\n\t"
    "v_mfma_f32_16x16x32_bf16 a[152:155], a[132:135], v[108:111], a[152:155]\n\t"
    "buffer_load_dwordx4 %[dv0], %[hsrd], s61 offen lds\n\t"
    "v_mfma_f32_16x16x32_bf16 a[156:159], a[132:135], v[112:115], a[156:159]\n\t"
    "v_exp_f32_e32 v34, v34\n\t"
    "v_mfma_f32_16x16x32_bf16 a[160:163], a[132:135], v[116:119], a[160:163]\n\t"
    "v_cvt_pk_bf16_f32 v94, v32, v33\n\t"
    "v_mfma_f32_16x16x32_bf16 a[164:167], a[136:139], v[100:103], a[164:167]\n\t"
    "v_exp_f32_e32 v35, v35\n\t"
    "v_mfma_f32_16x16x32_bf16 a[168:171], a[136:139], v[104:107], a[168:171]\n\t"
    "v_add_u32_e32 v221, s64, %[vb0]\n\t"
    "v_add_u32_e32 v222, s64, %[vb1]\n\t"
    "v_mfma_f32_16x16x32_bf16 a[172:175], a[136:139], v[108:111], a[172:175]\n\t"
    "v_exp_f32_e32 v36, v36\n\t"
    "v_mfma_f32_16x16x32_bf16 a[176:179], a[136:139], v[112:115], a[176:179]\n\t"
    "s_waitcnt lgkmcnt(0)\n\t"
    "v_cvt_pk_bf16_f32 v95, v34, v35\n\t"
    "v_mfma_f32_16x16x32_bf16 a[180:183], a[136:139], v[116:119], a[180:183]\n\t"
    "v_exp_f32_e32 v37, v37\n\t"
    "v_mfma_f32_16x16x32_bf16 a[184:187], a[140:143], v[100:103], a[184:187]\n\t"
    "buffer_load_dwordx4 %[dv1], %[hsrd], s61 offen offset:1024 lds\n\t"
    "v_mfma_f32_16x16x32_bf16 a[188:191], a[140:143], v[104:107], a[188:191]\n\t"
    "v_exp_f32_e32 v38, v38\n\t"
    "v_mfma_f32_16x16x32_bf16 a[192:195], a[140:143], v[108:111], a[192:195]\n\t"
    "v_cvt_pk_bf16_f32 v98, v36, v37\n\t"
    "v_mfma_f32_16x16x32_bf16 a[196:199], a[140:143], v[112:115], a[196:199]\n\t"
    "v_exp_f32_e32 v39, v39\n\t"
    "v_mfma_f32_16x16x32_bf16 a[200:203], a[140:143], v[116:119], a[200:203]\n\t"
    "s_sub_u32 s60, s60, 1\n\t"
    "s_cbranch_scc0 L_attn_loop_%=\n\t"
    "v_mfma_f32_16x16x32_bf16 v[0:3], a[80:83], a[0:3], 0\n\t"
    "v_exp_f32_e32 v40, v40\n\t"
    "v_mfma_f32_16x16x32_bf16 v[0:3], a[84:87], a[4:7], v[0:3]\n\t"
    "ds_read_b64_tr_b16 a[112:113], v221 offset:0\n\t"
    "v_cvt_pk_bf16_f32 v99, v38, v39\n\t"
    "v_mfma_f32_16x16x32_bf16 v[0:3], a[88:91], a[8:11], v[0:3]\n\t"
    "v_exp_f32_e32 v41, v41\n\t"
    "v_mfma_f32_16x16x32_bf16 v[0:3], a[92:95], a[12:15], v[0:3]\n\t"
    "ds_read_b64_tr_b16 a[114:115], v221 offset:4096\n\t"
    "v_mfma_f32_16x16x32_bf16 v[4:7], a[80:83], a[16:19], 0\n\t"
    "v_exp_f32_e32 v42, v42\n\t"
    "v_mfma_f32_16x16x32_bf16 v[4:7], a[84:87], a[20:23], v[4:7]\n\t"
    "ds_read_b64_tr_b16 a[116:117], v222 offset:0\n\t"
    "v_cvt_pk_bf16_f32 v100, v40, v41\n\t"
    "v_mfma_f32_16x16x32_bf16 v[4:7], a[88:91], a[24:27], v[4:7]\n\t"
    "v_exp_f32_e32 v43, v43\n\t"
    "v_mfma_f32_16x16x32_bf16 v[4:7], a[92:95], a[28:31], v[4:7]\n\t"
    "ds_read_b64_tr_b16 a[118:119], v222 offset:4096\n\t"
    "v_mfma_f32_16x16x32_bf16 v[8:11], a[80:83], a[32:35], 0\n\t"
    "v_exp_f32_e32 v44, v44\n\t"
    "v_mfma_f32_16x16x32_bf16 v[8:11], a[84:87], a[36:39], v[8:11]\n\t"
    "ds_read_b64_tr_b16 a[120:121], v221 offset:1024\n\t"
    "v_cvt_pk_bf16_f32 v101, v42, v43\n\t"
    "v_mfma_f32_16x16x32_bf16 v[8:11], a[88:91], a[40:43], v[8:11]\n\t"
    "v_exp_f32_e32 v45, v45\n\t"
    "v_mfma_f32_16x16x32_bf16 v[8:11], a[92:95], a[44:47], v[8:11]\n\t"
    "ds_read_b64_tr_b16 a[122:123], v221 offset:5120\n\t"
    "v_mfma_f32_16x16x32_bf16 v[12:15], a[80:83], a[48:51], 0\n\t"
    "v_exp_f32_e32 v46, v46\n\t"
    "v_mfma_f32_16x16x32_bf16 v[12:15], a[84:87], a[52:55], v[12:15]\n\t"
    "ds_read_b64_tr_b16 a[124:125], v222 offset:1024\n\t"
    "v_cvt_pk_bf16_f32 v104, v44, v45\n\t"
    "v_mfma_f32_16x16x32_bf16 v[12:15], a[88:91], a[56:59], v[12:15]\n\t"
    "v_exp_f32_e32 v47, v47\n\t"
    "v_mfma_f32_16x16x32_bf16 v[12:15], a[92:95], a[60:63], v[12:15]\n\t"
    "ds_read_b64_tr_b16 a[126:127], v222 offset:5120\n\t"
    "v_mfma_f32_16x16x32_bf16 v[16:19], a[80:83], a[64:67], 0\n\t"
    "v_exp_f32_e32 v48, v48\n\t"
    "v_mfma_f32_16x16x32_bf16 v[16:19], a[84:87], a[68:71], v[16:19]\n\t"
    "ds_read_b64_tr_b16 a[128:129], v221 offset:2048\n\t"
    "v_cvt_pk_bf16_f32 v105, v46, v47\n\t"
    "v_mfma_f32_16x16x32_bf16 v[16:19], a[88:91], a[72:75], v[16:19]\n\t"
    "v_exp_f32_e32 v49, v49\n\t"
    "v_mfma_f32_16x16x32_bf16 v[16:19], a[92:95], a[76:79], v[16:19]\n\t"
    "ds_read_b64_tr_b16 a[130:131], v221 offset:6144\n\t"
    "v_mfma_f32_16x16x32_bf16 v[20:23], a[96:99], a[0:3], 0\n\t"
    "v_exp_f32_e32 v50, v50\n\t"
    "v_mfma_f32_16x16x32_bf16 v[20:23], a[100:103], a[4:7], v[20:23]\n\t"
    "ds_read_b64_tr_b16 a[132:133], v222 offset:2048\n\t"
    "v_cvt_pk_bf16_f32 v108, v48, v49\n\t"
    "v_mfma_f32_16x16x32_bf16 v[20:23], a[104:107], a[8:11], v[20:23]\n\t"
    "v_exp_f32_e32 v51, v51\n\t"
    "v_mfma_f32_16x16x32_bf16 v[20:23], a[108:111], a[12:15], v[20:23]\n\t"
    "ds_read_b64_tr_b16 a[134:135], v222 offset:6144\n\t"
    "s_waitcnt lgkmcnt(4)\n\t"
    "v_mfma_f32_16x16x32_bf16 v[24:27], a[96:99], a[16:19], 0\n\t"
    "v_exp_f32_e32 v52, v52\n\t"
    "v_mfma_f32_16x16x32_bf16 v[24:27], a[100:103], a[20:23], v[24:27]\n\t"
    "ds_read_b64_tr_b16 a[136:137], v221 offset:3072\n\t"
    "v_cvt_pk_bf16_f32 v109, v50, v51\n\t"
    "v_mfma_f32_16x16x32_bf16 v[24:27], a[104:107], a[24:27], v[24:27]\n\t"
    "v_exp_f32_e32 v53, v53\n\t"
    "v_mfma_f32_16x16x32_bf16 v[24:27], a[108:111], a[28:31], v[24:27]\n\t"
    "ds_read_b64_tr_b16 a[138:139], v221 offset:7168\n\t"
    "v_mfma_f32_16x16x32_bf16 v[28:31], a[96:99], a[32:35], 0\n\t"
    "v_exp_f32_e32 v54, v54\n\t"
    "v_mfma_f32_16x16x32_bf16 v[28:31], a[100:103], a[36:39], v[28:31]\n\t"
    "ds_read_b64_tr_b16 a[140:141], v222 offset:3072\n\t"
    "v_cvt_pk_bf16_f32 v112, v52, v53\n\t"
    "v_mfma_f32_16x16x32_bf16 v[28:31], a[104:107], a[40:43], v[28:31]\n\t"
    "v_exp_f32_e32 v55, v55\n\t"
    "v_mfma_f32_16x16x32_bf16 v[28:31], a[108:111], a[44:47], v[28:31]\n\t"
    "ds_read_b64_tr_b16 a[142:143], v222 offset:7168\n\t"
    "v_mfma_f32_16x16x32_bf16 v[32:35], a[96:99], a[48:51], 0\n\t"
    "v_exp_f32_e32 v56, v56\n\t"
    "v_mfma_f32_16x16x32_bf16 v[32:35], a[100:103], a[52:55], v[32:35]\n\t"
    "v_cvt_pk_bf16_f32 v113, v54, v55\n\t"
    "v_mfma_f32_16x16x32_bf16 v[32:35], a[104:107], a[56:59], v[32:35]\n\t"
    "v_exp_f32_e32 v57, v57\n\t"
    "v_mfma_f32_16x16x32_bf16 v[32:35], a[108:111], a[60:63], v[32:35]\n\t"
    "v_mfma_f32_16x16x32_bf16 v[36:39], a[96:99], a[64:67], 0\n\t"
    "v_exp_f32_e32 v58, v58\n\t"
    "v_mfma_f32_16x16x32_bf16 v[36:39], a[100:103], a[68:71], v[36:39]\n\t"
    "v_cvt_pk_bf16_f32 v116, v56, v57\n\t"
    "v_mfma_f32_16x16x32_bf16 v[36:39], a[104:107], a[72:75], v[36:39]\n\t"
    "v_exp_f32_e32 v59, v59\n\t"
    "v_mfma_f32_16x16x32_bf16 v[36:39], a[108:111], a[76:79], v[36:39]\n\t"
    "v_mfma_f32_16x16x32_bf16 v[120:123], a[112:115], v[80:83], v[120:123]\n\t"
    "v_exp_f32_e32 v60, v60\n\t"
    "v_mfma_f32_16x16x32_bf16 v[124:127], a[112:115], v[84:87], v[124:127]\n\t"
    "v_cvt_pk_bf16_f32 v117, v58, v59\n\t"
    "v_mfma_f32_16x16x32_bf16 v[128:131], a[112:115], v[88:91], v[128:131]\n\t"
    "v_exp_f32_e32 v61, v61\n\t"
    "v_mfma_f32_16x16x32_bf16 v[132:135], a[112:115], v[92:95], v[132:135]\n\t"
    "v_mfma_f32_16x16x32_bf16 v[136:139], a[112:115], v[96:99], v[136:139]\n\t"
    "v_exp_f32_e32 v62, v62\n\t"
    "v_mfma_f32_16x16x32_bf16 v[140:143], a[116:119], v[80:83], v[140:143]\n\t"
    "v_cvt_pk_bf16_f32 v102, v60, v61\n\t"
    "v_mfma_f32_16x16x32_bf16 v[144:147], a[116:119], v[84:87], v[144:147]\n\t"
    "v_exp_f32_e32 v63, v63\n\t"
    "v_mfma_f32_16x16x32_bf16 v[148:151], a[116:119], v[88:91], v[148:151]\n\t"
    "v_mfma_f32_16x16x32_bf16 v[152:155], a[116:119], v[92:95], v[152:155]\n\t"
    "v_exp_f32_e32 v64, v64\n\t"
    "v_mfma_f32_16x16x32_bf16 v[156:159], a[116:119], v[96:99], v[156:159]\n\t"
    "v_cvt_pk_bf16_f32 v103, v62, v63\n\t"
    "v_mfma_f32_16x16x32_bf16 v[160:163], a[120:123], v[80:83], v[160:163]\n\t"
    "v_exp_f32_e32 v65, v65\n\t"
    "v_mfma_f32_16x16x32_bf16 v[164:167], a[120:123], v[84:87], v[164:167]\n\t"
    "s_waitcnt lgkmcnt(0)\n\t"
    "v_mfma_f32_16x16x32_bf16 v[168:171], a[120:123], v[88:91], v[168:171]\n\t"
    "v_exp_f32_e32 v66, v66\n\t"
    "v_mfma_f32_16x16x32_bf16 v[172:175], a[120:123], v[92:95], v[172:175]\n\t"
    "v_cvt_pk_bf16_f32 v106, v64, v65\n\t"
    "v_mfma_f32_16x16x32_bf16 v[176:179], a[120:123], v[96:99], v[176:179]\n\t"
    "v_exp_f32_e32 v67, v67\n\t"
    "v_mfma_f32_16x16x32_bf16 v[180:183], a[124:127], v[80:83], v[180:183]\n\t"
    "v_mfma_f32_16x16x32_bf16 v[184:187], a[124:127], v[84:87], v[184:187]\n\t"
    "v_exp_f32_e32 v68, v68\n\t"
    "v_mfma_f32_16x16x32_bf16 v[188:191], a[124:127], v[88:91], v[188:191]\n\t"
    "v_cvt_pk_bf16_f32 v107, v66, v67\n\t"
    "v_mfma_f32_16x16x32_bf16 v[192:195], a[124:127], v[92:95], v[192:195]\n\t"
    "v_exp_f32_e32 v69, v69\n\t"
    "v_mfma_f32_16x16x32_bf16 v[196:199], a[124:127], v[96:99], v[196:199]\n\t"
    "v_mfma_f32_16x16x32_bf16 v[200:203], a[128:131], v[80:83], v[200:203]\n\t"
    "v_exp_f32_e32 v70, v70\n\t"
    "v_mfma_f32_16x16x32_bf16 v[204:207], a[128:131], v[84:87], v[204:207]\n\t"
    "v_cvt_pk_bf16_f32 v110, v68, v69\n\t"
    "v_mfma_f32_16x16x32_bf16 v[208:211], a[128:131], v[88:91], v[208:211]\n\t"
    "v_exp_f32_e32 v71, v71\n\t"
    "v_mfma_f32_16x16x32_bf16 v[212:215], a[128:131], v[92:95], v[212:215]\n\t"
    "v_mfma_f32_16x16x32_bf16 v[216:219], a[128:131], v[96:99], v[216:219]\n\t"
    "v_exp_f32_e32 v72, v72\n\t"
    "v_mfma_f32_16x16x32_bf16 a[144:147], a[132:135], v[80:83], a[144:147]\n\t"
    "v_cvt_pk_bf16_f32 v111, v70, v71\n\t"
    "v_mfma_f32_16x16x32_bf16 a[148:151], a[132:135], v[84:87], a[148:151]\n\t"
    "v_exp_f32_e32 v73, v73\n\t"
    "v_mfma_f32_16x16x32_bf16 a[152:155], a[132:135], v[88:91], a[152:155]\n\t"
    "v_mfma_f32_16x16x32_bf16 a[156:159], a[132:135], v[92:95], a[156:159]\n\t"
    "v_exp_f32_e32 v74, v74\n\t"
    "v_mfma_f32_16x16x32_bf16 a[160:163], a[132:135], v[96:99], a[160:163]\n\t"
    "v_cvt_pk_bf16_f32 v114, v72, v73\n\t"
    "v_mfma_f32_16x16x32_bf16 a[164:167], a[136:139], v[80:83], a[164:167]\n\t"
    "v_exp_f32_e32 v75, v75\n\t"
    "v_mfma_f32_16x16x32_bf16 a[168:171], a[136:139], v[84:87], a[168:171]\n\t"
    "v_mfma_f32_16x16x32_bf16 a[172:175], a[136:139], v[88:91], a[172:175]\n\t"
    "v_exp_f32_e32 v76, v76\n\t"
    "v_mfma_f32_16x16x32_bf16 a[176:179], a[136:139], v[92:95], a[176:179]\n\t"
    "v_cvt_pk_bf16_f32 v115, v74, v75\n\t"
    "v_mfma_f32_16x16x32_bf16 a[180:183], a[136:139], v[96:99], a[180:183]\n\t"
    "v_exp_f32_e32 v77, v77\n\t"
    "v_mfma_f32_16x16x32_bf16 a[184:187], a[140:143], v[80:83], a[184:187]\n\t"
    "v_mfma_f32_16x16x32_bf16 a[188:191], a[140:143], v[84:87], a[188:191]\n\t"
    "v_exp_f32_e32 v78, v78\n\t"
    "v_mfma_f32_16x16x32_bf16 a[192:195], a[140:143], v[88:91], a[192:195]\n\t"
    "v_cvt_pk_bf16_f32 v118, v76, v77\n\t"
    "v_mfma_f32_16x16x32_bf16 a[196:199], a[140:143], v[92:95], a[196:199]\n\t"
    "v_exp_f32_e32 v79, v79\n\t"
    "v_mfma_f32_16x16x32_bf16 a[200:203], a[140:143], v[96:99], a[200:203]\n\t"
    "s_nop 1\n\t"
    "v_exp_f32_e32 v0, v0\n\t"
    "ds_read_b64_tr_b16 a[112:113], v221 offset:8192\n\t"
    "v_cvt_pk_bf16_f32 v119, v78, v79\n\t"
    "v_exp_f32_e32 v1, v1\n\t"
    "ds_read_b64_tr_b16 a[114:115], v221 offset:12288\n\t"
    "v_exp_f32_e32 v2, v2\n\t"
    "ds_read_b64_tr_b16 a[116:117], v222 offset:8192\n\t"
    "v_cvt_pk_bf16_f32 v80, v0, v1\n\t"
    "v_exp_f32_e32 v3, v3\n\t"
    "ds_read_b64_tr_b16 a[118:119], v222 offset:12288\n\t"
    "v_exp_f32_e32 v4, v4\n\t"
    "ds_read_b64_tr_b16 a[120:121], v221 offset:9216\n\t"
    "v_cvt_pk_bf16_f32 v81, v2, v3\n\t"
    "v_exp_f32_e32 v5, v5\n\t"
    "ds_read_b64_tr_b16 a[122:123], v221 offset:13312\n\t"
    "v_exp_f32_e32 v6, v6\n\t"
    "ds_read_b64_tr_b16 a[124:125], v222 offset:9216\n\t"
    "v_cvt_pk_bf16_f32 v84, v4, v5\n\t"
    "v_exp_f32_e32 v7, v7\n\t"
    "ds_read_b64_tr_b16 a[126:127], v222 offset:13312\n\t"
    "v_exp_f32_e32 v8, v8\n\t"
    "ds_read_b64_tr_b16 a[128:129], v221 offset:10240\n\t"
    "v_cvt_pk_bf16_f32 v85, v6, v7\n\t"
    "v_exp_f32_e32 v9, v9\n\t"
    "ds_read_b64_tr_b16 a[130:131], v221 offset:14336\n\t"
    "v_exp_f32_e32 v10, v10\n\t"
    "ds_read_b64_tr_b16 a[132:133], v222 offset:10240\n\t"
    "v_cvt_pk_bf16_f32 v88, v8, v9\n\t"
    "v_exp_f32_e32 v11, v11\n\t"
    "ds_read_b64_tr_b16 a[134:135], v222 offset:14336\n\t"
    "s_waitcnt lgkmcnt(4)\n\t"
    "v_exp_f32_e32 v12, v12\n\t"
    "ds_read_b64_tr_b16 a[136:137], v221 offset:11264\n\t"
    "v_cvt_pk_bf16_f32 v89, v10, v11\n\t"
    "v_exp_f32_e32 v13, v13\n\t"
    "ds_read_b64_tr_b16 a[138:139], v221 offset:15360\n\t"
    "v_exp_f32_e32 v14, v14\n\t"
    "ds_read_b64_tr_b16 a[140:141], v222 offset:11264\n\t"
    "v_cvt_pk_bf16_f32 v92, v12, v13\n\t"
    "v_exp_f32_e32 v15, v15\n\t"
    "ds_read_b64_tr_b16 a[142:143], v222 offset:15360\n\t"
    "v_exp_f32_e32 v16, v16\n\t"
    "v_cvt_pk_bf16_f32 v93, v14, v15\n\t"
    "v_exp_f32_e32 v17, v17\n\t"
    "v_exp_f32_e32 v18, v18\n\t"
    "v_cvt_pk_bf16_f32 v96, v16, v17\n\t"
    "v_exp_f32_e32 v19, v19\n\t"
    "v_mfma_f32_16x16x32_bf16 v[120:123], a[112:115], v[100:103], v[120:123]\n\t"
    "v_exp_f32_e32 v20, v20\n\t"
    "v_mfma_f32_16x16x32_bf16 v[124:127], a[112:115], v[104:107], v[124:127]\n\t"
    "v_cvt_pk_bf16_f32 v97, v18, v19\n\t"
    "v_mfma_f32_16x16x32_bf16 v[128:131], a[112:115], v[108:111], v[128:131]\n\t"
    "v_exp_f32_e32 v21, v21\n\t"
    "v_mfma_f32_16x16x32_bf16 v[132:135], a[112:115], v[112:115], v[132:135]\n\t"
    "v_mfma_f32_16x16x32_bf16 v[136:139], a[112:115], v[116:119], v[136:139]\n\t"
    "v_exp_f32_e32 v22, v22\n\t"
    "v_mfma_f32_16x16x32_bf16 v[140:143], a[116:119], v[100:103], v[140:143]\n\t"
    "v_cvt_pk_bf16_f32 v82, v20, v21\n\t"
    "v_mfma_f32_16x16x32_bf16 v[144:147], a[116:119], v[104:107], v[144:147]\n\t"
    "v_exp_f32_e32 v23, v23\n\t"
    "v_mfma_f32_16x16x32_bf16 v[148:151], a[116:119], v[108:111], v[148:151]\n\t"
    "v_mfma_f32_16x16x32_bf16 v[152:155], a[116:119], v[112:115], v[152:155]\n\t"
    "v_exp_f32_e32 v24, v24\n\t"
    "v_mfma_f32_16x16x32_bf16 v[156:159], a[116:119], v[116:119], v[156:159]\n\t"
    "v_cvt_pk_bf16_f32 v83, v22, v23\n\t"
    "v_mfma_f32_16x16x32_bf16 v[160:163], a[120:123], v[100:103], v[160:163]\n\t"
    "v_exp_f32_e32 v25, v25\n\t"
    "v_mfma_f32_16x16x32_bf16 v[164:167], a[120:123], v[104:107], v[164:167]\n\t"
    "s_waitcnt lgkmcnt(0)\n\t"
    "v_mfma_f32_16x16x32_bf16 v[168:171], a[120:123], v[108:111], v[168:171]\n\t"
    "v_exp_f32_e32 v26, v26\n\t"
    "v_mfma_f32_16x16x32_bf16 v[172:175], a[120:123], v[112:115], v[172:175]\n\t"
    "v_cvt_pk_bf16_f32 v86, v24, v25\n\t"
    "v_mfma_f32_16x16x32_bf16 v[176:179], a[120:123], v[116:119], v[176:179]\n\t"
    "v_exp_f32_e32 v27, v27\n\t"
    "v_mfma_f32_16x16x32_bf16 v[180:183], a[124:127], v[100:103], v[180:183]\n\t"
    "v_mfma_f32_16x16x32_bf16 v[184:187], a[124:127], v[104:107], v[184:187]\n\t"
    "v_exp_f32_e32 v28, v28\n\t"
    "v_mfma_f32_16x16x32_bf16 v[188:191], a[124:127], v[108:111], v[188:191]\n\t"
    "v_cvt_pk_bf16_f32 v87, v26, v27\n\t"
    "v_mfma_f32_16x16x32_bf16 v[192:195], a[124:127], v[112:115], v[192:195]\n\t"
    "v_exp_f32_e32 v29, v29\n\t"
    "v_mfma_f32_16x16x32_bf16 v[196:199], a[124:127], v[116:119], v[196:199]\n\t"
    "v_mfma_f32_16x16x32_bf16 v[200:203], a[128:131], v[100:103], v[200:203]\n\t"
    "v_exp_f32_e32 v30, v30\n\t"
    "v_mfma_f32_16x16x32_bf16 v[204:207], a[128:131], v[104:107], v[204:207]\n\t"
    "v_cvt_pk_bf16_f32 v90, v28, v29\n\t"
    "v_mfma_f32_16x16x32_bf16 v[208:211], a[128:131], v[108:111], v[208:211]\n\t"
    "v_exp_f32_e32 v31, v31\n\t"
    "v_mfma_f32_16x16x32_bf16 v[212:215], a[128:131], v[112:115], v[212:215]\n\t"
    "v_mfma_f32_16x16x32_bf16 v[216:219], a[128:131], v[116:119], v[216:219]\n\t"
    "v_exp_f32_e32 v32, v32\n\t"
    "v_mfma_f32_16x16x32_bf16 a[144:147], a[132:135], v[100:103], a[144:147]\n\t"
    "v_cvt_pk_bf16_f32 v91, v30, v31\n\t"
    "v_mfma_f32_16x16x32_bf16 a[148:151], a[132:135], v[104:107], a[148:151]\n\t"
    "v_exp_f32_e32 v33, v33\n\t"
    "v_mfma_f32_16x16x32_bf16 a[152:155], a[132:135], v[108:111], a[152:155]\n\t"
    "v_mfma_f32_16x16x32_bf16 a[156:159], a[132:135], v[112:115], a[156:159]\n\t"
    "v_exp_f32_e32 v34, v34\n\t"
    "v_mfma_f32_16x16x32_bf16 a[160:163], a[132:135], v[116:119], a[160:163]\n\t"
    "v_cvt_pk_bf16_f32 v94, v32, v33\n\t"
    "v_mfma_f32_16x16x32_bf16 a[164:167], a[136:139], v[100:103], a[164:167]\n\t"
    "v_exp_f32_e32 v35, v35\n\t"
    "v_mfma_f32_16x16x32_bf16 a[168:171], a[136:139], v[104:107], a[168:171]\n\t"
    "v_add_u32_e32 v221, s63, %[vb0]\n\t"
    "v_add_u32_e32 v222, s63, %[vb1]\n\t"
    "v_mfma_f32_16x16x32_bf16 a[172:175], a[136:139], v[108:111], a[172:175]\n\t"
    "v_exp_f32_e32 v36, v36\n\t"
    "v_mfma_f32_16x16x32_bf16 a[176:179], a[136:139], v[112:115], a[176:179]\n\t"
    "v_cvt_pk_bf16_f32 v95, v34, v35\n\t"
    "v_mfma_f32_16x16x32_bf16 a[180:183], a[136:139], v[116:119], a[180:183]\n\t"
    "v_exp_f32_e32 v37, v37\n\t"
    "v_mfma_f32_16x16x32_bf16 a[184:187], a[140:143], v[100:103], a[184:187]\n\t"
    "v_mfma_f32_16x16x32_bf16 a[188:191], a[140:143], v[104:107], a[188:191]\n\t"
    "v_exp_f32_e32 v38, v38\n\t"
    "v_mfma_f32_16x16x32_bf16 a[192:195], a[140:143], v[108:111], a[192:195]\n\t"
    "v_cvt_pk_bf16_f32 v98, v36, v37\n\t"
    "v_mfma_f32_16x16x32_bf16 a[196:199], a[140:143], v[112:115], a[196:199]\n\t"
    "v_exp_f32_e32 v39, v39\n\t"
    "v_mfma_f32_16x16x32_bf16 a[200:203], a[140:143], v[116:119], a[200:203]\n\t"
    "s_nop 1\n\t"
    "ds_read_b64_tr_b16 a[112:113], v221 offset:0\n\t"
    "v_cvt_pk_bf16_f32 v99, v38, v39\n\t"
    "ds_read_b64_tr_b16 a[114:115], v221 offset:4096\n\t"
    "ds_read_b64_tr_b16 a[116:117], v222 offset:0\n\t"
    "ds_read_b64_tr_b16 a[118:119], v222 offset:4096\n\t"
    "ds_read_b64_tr_b16 a[120:121], v221 offset:1024\n\t"
    "ds_read_b64_tr_b16 a[122:123], v221 offset:5120\n\t"
    "ds_read_b64_tr_b16 a[124:125], v222 offset:1024\n\t"
    "ds_read_b64_tr_b16 a[126:127], v222 offset:5120\n\t"
    "ds_read_b64_tr_b16 a[128:129], v221 offset:2048\n\t"
    "ds_read_b64_tr_b16 a[130:131], v221 offset:6144\n\t"
    "ds_read_b64_tr_b16 a[132:133], v222 offset:2048\n\t"
    "ds_read_b64_tr_b16 a[134:135], v222 offset:6144\n\t"
    "ds_read_b64_tr_b16 a[136:137], v221 offset:3072\n\t"
    "ds_read_b64_tr_b16 a[138:139], v221 offset:7168\n\t"
    "ds_read_b64_tr_b16 a[140:141], v222 offset:3072\n\t"
    "ds_read_b64_tr_b16 a[142:143], v222 offset:7168\n\t"
    "s_add_u32 s84, %[pooff], 0\n\t"
    "s_add_u32 s85, %[pooff], 28672\n\t"
    "s_add_u32 s86, %[pooff], 57344\n\t"
    "s_add_u32 s87, %[pooff], 86016\n\t"
    "s_add_u32 s88, %[pooff], 114688\n\t"
    "s_waitcnt lgkmcnt(0)\n\t"
    "v_mfma_f32_16x16x32_bf16 v[120:123], a[112:115], v[80:83], v[120:123]\n\t"
    "v_mfma_f32_16x16x32_bf16 v[140:143], a[116:119], v[80:83], v[140:143]\n\t"
    "v_mfma_f32_16x16x32_bf16 v[160:163], a[120:123], v[80:83], v[160:163]\n\t"
    "v_mfma_f32_16x16x32_bf16 v[180:183], a[124:127], v[80:83], v[180:183]\n\t"
    "v_mfma_f32_16x16x32_bf16 v[200:203], a[128:131], v[80:83], v[200:203]\n\t"
    "v_mfma_f32_16x16x32_bf16 a[144:147], a[132:135], v[80:83], a[144:147]\n\t"
    "v_mfma_f32_16x16x32_bf16 a[164:167], a[136:139], v[80:83], a[164:167]\n\t"
    "v_mfma_f32_16x16x32_bf16 a[184:187], a[140:143], v[80:83], a[184:187]\n\t"
    "v_mfma_f32_16x16x32_bf16 v[124:127], a[112:115], v[84:87], v[124:127]\n\t"
    "v_mfma_f32_16x16x32_bf16 v[144:147], a[116:119], v[84:87], v[144:147]\n\t"
    "v_mfma_f32_16x16x32_bf16 v[164:167], a[120:123], v[84:87], v[164:167]\n\t"
    "v_mfma_f32_16x16x32_bf16 v[184:187], a[124:127], v[84:87], v[184:187]\n\t"
    "v_mfma_f32_16x16x32_bf16 v[204:207], a[128:131], v[84:87], v[204:207]\n\t"
    "v_mfma_f32_16x16x32_bf16 a[148:151], a[132:135], v[84:87], a[148:151]\n\t"
    "v_mfma_f32_16x16x32_bf16 a[168:171], a[136:139], v[84:87], a[168:171]\n\t"
    "v_mfma_f32_16x16x32_bf16 a[188:191], a[140:143], v[84:87], a[188:191]\n\t"
    "v_cvt_pk_bf16_f32 v0, v120, v121\n\t"
    "v_cvt_pk_bf16_f32 v1, v122, v123\n\t"
    "v_cvt_pk_bf16_f32 v2, v140, v141\n\t"
    "v_cvt_pk_bf16_f32 v3, v142, v143\n\t"
    "v_cvt_pk_bf16_f32 v4, v160, v161\n\t"
    "v_cvt_pk_bf16_f32 v5, v162, v163\n\t"
    "v_cvt_pk_bf16_f32 v6, v180, v181\n\t"
    "v_cvt_pk_bf16_f32 v7, v182, v183\n\t"
    "v_cvt_pk_bf16_f32 v8, v200, v201\n\t"
    "v_cvt_pk_bf16_f32 v9, v202, v203\n\t"
    "v_accvgpr_read_b32 v223, a144\n\t"
    "v_accvgpr_read_b32 v224, a145\n\t"
    "v_accvgpr_read_b32 v225, a146\n\t"
    "v_accvgpr_read_b32 v226, a147\n\t"
    "v_cvt_pk_bf16_f32 v10, v223, v224\n\t"
    "v_cvt_pk_bf16_f32 v11, v225, v226\n\t"
    "v_accvgpr_read_b32 v223, a164\n\t"
    "v_accvgpr_read_b32 v224, a165\n\t"
    "v_accvgpr_read_b32 v225, a166\n\t"
    "v_accvgpr_read_b32 v226, a167\n\t"
    "v_cvt_pk_bf16_f32 v12, v223, v224\n\t"
    "v_cvt_pk_bf16_f32 v13, v225, v226\n\t"
    "v_accvgpr_read_b32 v223, a184\n\t"
    "v_accvgpr_read_b32 v224, a185\n\t"
    "v_accvgpr_read_b32 v225, a186\n\t"
    "v_accvgpr_read_b32 v226, a187\n\t"
    "v_cvt_pk_bf16_f32 v14, v223, v224\n\t"
    "v_cvt_pk_bf16_f32 v15, v225, v226\n\t"
    "s_cmp_eq_u32 %[own0], 0\n\t"
    "s_cbranch_scc1 L_glob0_%=\n\t"
    "v_add_u32_e32 v223, %[own0], %[lane16]\n\t"
    "ds_write_b128 v223, v[0:3] offset:0\n\t"
    "ds_write_b128 v223, v[4:7] offset:1024\n\t"
    "ds_write_b128 v223, v[8:11] offset:2048\n\t"
    "ds_write_b128 v223, v[12:15] offset:3072\n\t"
    "s_branch L_left0_%=\n\t"
    "L_glob0_%=:\n\t"
    "buffer_store_dwordx4 v[0:3], %[lane16], %[srd], s84 offen sc1\n\t"
    "buffer_store_dwordx4 v[4:7], %[lane16], %[srd], s84 offen offset:1024 sc1\n\t"
    "buffer_store_dwordx4 v[8:11], %[lane16], %[srd], s84 offen offset:2048 sc1\n\t"
    "buffer_store_dwordx4 v[12:15], %[lane16], %[srd], s84 offen offset:3072 sc1\n\t"
    "L_left0_%=:\n\t"
    "v_mfma_f32_16x16x32_bf16 v[128:131], a[112:115], v[88:91], v[128:131]\n\t"
    "v_mfma_f32_16x16x32_bf16 v[148:151], a[116:119], v[88:91], v[148:151]\n\t"
    "v_mfma_f32_16x16x32_bf16 v[168:171], a[120:123], v[88:91], v[168:171]\n\t"
    "v_mfma_f32_16x16x32_bf16 v[188:191], a[124:127], v[88:91], v[188:191]\n\t"
    "v_mfma_f32_16x16x32_bf16 v[208:211], a[128:131], v[88:91], v[208:211]\n\t"
    "v_mfma_f32_16x16x32_bf16 a[152:155], a[132:135], v[88:91], a[152:155]\n\t"
    "v_mfma_f32_16x16x32_bf16 a[172:175], a[136:139], v[88:91], a[172:175]\n\t"
    "v_mfma_f32_16x16x32_bf16 a[192:195], a[140:143], v[88:91], a[192:195]\n\t"
    "v_cvt_pk_bf16_f32 v16, v124, v125\n\t"
    "v_cvt_pk_bf16_f32 v17, v126, v127\n\t"
    "v_cvt_pk_bf16_f32 v18, v144, v145\n\t"
    "v_cvt_pk_bf16_f32 v19, v146, v147\n\t"
    "v_cvt_pk_bf16_f32 v20, v164, v165\n\t"
    "v_cvt_pk_bf16_f32 v21, v166, v167\n\t"
    "v_cvt_pk_bf16_f32 v22, v184, v185\n\t"
    "v_cvt_pk_bf16_f32 v23, v186, v187\n\t"
    "v_cvt_pk_bf16_f32 v24, v204, v205\n\t"
    "v_cvt_pk_bf16_f32 v25, v206, v207\n\t"
    "v_accvgpr_read_b32 v223, a148\n\t"
    "v_accvgpr_read_b32 v224, a149\n\t"
    "v_accvgpr_read_b32 v225, a150\n\t"
    "v_accvgpr_read_b32 v226, a151\n\t"
    "v_cvt_pk_bf16_f32 v26, v223, v224\n\t"
    "v_cvt_pk_bf16_f32 v27, v225, v226\n\t"
    "v_accvgpr_read_b32 v223, a168\n\t"
    "v_accvgpr_read_b32 v224, a169\n\t"
    "v_accvgpr_read_b32 v225, a170\n\t"
    "v_accvgpr_read_b32 v226, a171\n\t"
    "v_cvt_pk_bf16_f32 v28, v223, v224\n\t"
    "v_cvt_pk_bf16_f32 v29, v225, v226\n\t"
    "v_accvgpr_read_b32 v223, a188\n\t"
    "v_accvgpr_read_b32 v224, a189\n\t"
    "v_accvgpr_read_b32 v225, a190\n\t"
    "v_accvgpr_read_b32 v226, a191\n\t"
    "v_cvt_pk_bf16_f32 v30, v223, v224\n\t"
    "v_cvt_pk_bf16_f32 v31, v225, v226\n\t"
    "s_cmp_eq_u32 %[own1], 0\n\t"
    "s_cbranch_scc1 L_glob1_%=\n\t"
    "v_add_u32_e32 v223, %[own1], %[lane16]\n\t"
    "ds_write_b128 v223, v[16:19] offset:0\n\t"
    "ds_write_b128 v223, v[20:23] offset:1024\n\t"
    "ds_write_b128 v223, v[24:27] offset:2048\n\t"
    "ds_write_b128 v223, v[28:31] offset:3072\n\t"
    "s_branch L_left1_%=\n\t"
    "L_glob1_%=:\n\t"
    "buffer_store_dwordx4 v[16:19], %[lane16], %[srd], s85 offen sc1\n\t"
    "buffer_store_dwordx4 v[20:23], %[lane16], %[srd], s85 offen offset:1024 sc1\n\t"
    "buffer_store_dwordx4 v[24:27], %[lane16], %[srd], s85 offen offset:2048 sc1\n\t"
    "buffer_store_dwordx4 v[28:31], %[lane16], %[srd], s85 offen offset:3072 sc1\n\t"
    "L_left1_%=:\n\t"
    "v_mfma_f32_16x16x32_bf16 v[132:135], a[112:115], v[92:95], v[132:135]\n\t"
    "v_mfma_f32_16x16x32_bf16 v[152:155], a[116:119], v[92:95], v[152:155]\n\t"
    "v_mfma_f32_16x16x32_bf16 v[172:175], a[120:123], v[92:95], v[172:175]\n\t"
    "v_mfma_f32_16x16x32_bf16 v[192:195], a[124:127], v[92:95], v[192:195]\n\t"
    "v_mfma_f32_16x16x32_bf16 v[212:215], a[128:131], v[92:95], v[212:215]\n\t"
    "v_mfma_f32_16x16x32_bf16 a[156:159], a[132:135], v[92:95], a[156:159]\n\t"
    "v_mfma_f32_16x16x32_bf16 a[176:179], a[136:139], v[92:95], a[176:179]\n\t"
    "v_mfma_f32_16x16x32_bf16 a[196:199], a[140:143], v[92:95], a[196:199]\n\t"
    "v_cvt_pk_bf16_f32 v32, v128, v129\n\t"
    "v_cvt_pk_bf16_f32 v33, v130, v131\n\t"
    "v_cvt_pk_bf16_f32 v34, v148, v149\n\t"
    "v_cvt_pk_bf16_f32 v35, v150, v151\n\t"
    "v_cvt_pk_bf16_f32 v36, v168, v169\n\t"
    "v_cvt_pk_bf16_f32 v37, v170, v171\n\t"
    "v_cvt_pk_bf16_f32 v38, v188, v189\n\t"
    "v_cvt_pk_bf16_f32 v39, v190, v191\n\t"
    "v_cvt_pk_bf16_f32 v40, v208, v209\n\t"
    "v_cvt_pk_bf16_f32 v41, v210, v211\n\t"
    "v_accvgpr_read_b32 v223, a152\n\t"
    "v_accvgpr_read_b32 v224, a153\n\t"
    "v_accvgpr_read_b32 v225, a154\n\t"
    "v_accvgpr_read_b32 v226, a155\n\t"
    "v_cvt_pk_bf16_f32 v42, v223, v224\n\t"
    "v_cvt_pk_bf16_f32 v43, v225, v226\n\t"
    "v_accvgpr_read_b32 v223, a172\n\t"
    "v_accvgpr_read_b32 v224, a173\n\t"
    "v_accvgpr_read_b32 v225, a174\n\t"
    "v_accvgpr_read_b32 v226, a175\n\t"
    "v_cvt_pk_bf16_f32 v44, v223, v224\n\t"
    "v_cvt_pk_bf16_f32 v45, v225, v226\n\t"
    "v_accvgpr_read_b32 v223, a192\n\t"
    "v_accvgpr_read_b32 v224, a193\n\t"
    "v_accvgpr_read_b32 v225, a194\n\t"
    "v_accvgpr_read_b32 v226, a195\n\t"
    "v_cvt_pk_bf16_f32 v46, v223, v224\n\t"
    "v_cvt_pk_bf16_f32 v47, v225, v226\n\t"
    "s_cmp_eq_u32 %[own2], 0\n\t"
    "s_cbranch_scc1 L_glob2_%=\n\t"
    "v_add_u32_e32 v223, %[own2], %[lane16]\n\t"
    "ds_write_b128 v223, v[32:35] offset:0\n\t"
    "ds_write_b128 v223, v[36:39] offset:1024\n\t"
    "ds_write_b128 v223, v[40:43] offset:2048\n\t"
    "ds_write_b128 v223, v[44:47] offset:3072\n\t"
    "s_branch L_left2_%=\n\t"
    "L_glob2_%=:\n\t"
    "buffer_store_dwordx4 v[32:35], %[lane16], %[srd], s86 offen sc1\n\t"
    "buffer_store_dwordx4 v[36:39], %[lane16], %[srd], s86 offen offset:1024 sc1\n\t"
    "buffer_store_dwordx4 v[40:43], %[lane16], %[srd], s86 offen offset:2048 sc1\n\t"
    "buffer_store_dwordx4 v[44:47], %[lane16], %[srd], s86 offen offset:3072 sc1\n\t"
    "L_left2_%=:\n\t"
    "v_mfma_f32_16x16x32_bf16 v[136:139], a[112:115], v[96:99], v[136:139]\n\t"
    "v_mfma_f32_16x16x32_bf16 v[156:159], a[116:119], v[96:99], v[156:159]\n\t"
    "v_mfma_f32_16x16x32_bf16 v[176:179], a[120:123], v[96:99], v[176:179]\n\t"
    "v_mfma_f32_16x16x32_bf16 v[196:199], a[124:127], v[96:99], v[196:199]\n\t"
    "v_mfma_f32_16x16x32_bf16 v[216:219], a[128:131], v[96:99], v[216:219]\n\t"
    "v_mfma_f32_16x16x32_bf16 a[160:163], a[132:135], v[96:99], a[160:163]\n\t"
    "v_mfma_f32_16x16x32_bf16 a[180:183], a[136:139], v[96:99], a[180:183]\n\t"
    "v_mfma_f32_16x16x32_bf16 a[200:203], a[140:143], v[96:99], a[200:203]\n\t"
    "v_cvt_pk_bf16_f32 v48, v132, v133\n\t"
    "v_cvt_pk_bf16_f32 v49, v134, v135\n\t"
    "v_cvt_pk_bf16_f32 v50, v152, v153\n\t"
    "v_cvt_pk_bf16_f32 v51, v154, v155\n\t"
    "v_cvt_pk_bf16_f32 v52, v172, v173\n\t"
    "v_cvt_pk_bf16_f32 v53, v174, v175\n\t"
    "v_cvt_pk_bf16_f32 v54, v192, v193\n\t"
    "v_cvt_pk_bf16_f32 v55, v194, v195\n\t"
    "v_cvt_pk_bf16_f32 v56, v212, v213\n\t"
    "v_cvt_pk_bf16_f32 v57, v214, v215\n\t"
    "v_accvgpr_read_b32 v223, a156\n\t"
    "v_accvgpr_read_b32 v224, a157\n\t"
    "v_accvgpr_read_b32 v225, a158\n\t"
    "v_accvgpr_read_b32 v226, a159\n\t"
    "v_cvt_pk_bf16_f32 v58, v223, v224\n\t"
    "v_cvt_pk_bf16_f32 v59, v225, v226\n\t"
    "v_accvgpr_read_b32 v223, a176\n\t"
    "v_accvgpr_read_b32 v224, a177\n\t"
    "v_accvgpr_read_b32 v225, a178\n\t"
    "v_accvgpr_read_b32 v226, a179\n\t"
    "v_cvt_pk_bf16_f32 v60, v223, v224\n\t"
    "v_cvt_pk_bf16_f32 v61, v225, v226\n\t"
    "v_accvgpr_read_b32 v223, a196\n\t"
    "v_accvgpr_read_b32 v224, a197\n\t"
    "v_accvgpr_read_b32 v225, a198\n\t"
    "v_accvgpr_read_b32 v226, a199\n\t"
    "v_cvt_pk_bf16_f32 v62, v223, v224\n\t"
    "v_cvt_pk_bf16_f32 v63, v225, v226\n\t"
    "s_cmp_eq_u32 %[own3], 0\n\t"
    "s_cbranch_scc1 L_glob3_%=\n\t"
    "v_add_u32_e32 v223, %[own3], %[lane16]\n\t"
    "ds_write_b128 v223, v[48:51] offset:0\n\t"
    "ds_write_b128 v223, v[52:55] offset:1024\n\t"
    "ds_write_b128 v223, v[56:59] offset:2048\n\t"
    "ds_write_b128 v223, v[60:63] offset:3072\n\t"
    "s_branch L_left3_%=\n\t"
    "L_glob3_%=:\n\t"
    "buffer_store_dwordx4 v[48:51], %[lane16], %[srd], s87 offen sc1\n\t"
    "buffer_store_dwordx4 v[52:55], %[lane16], %[srd], s87 offen offset:1024 sc1\n\t"
    "buffer_store_dwordx4 v[56:59], %[lane16], %[srd], s87 offen offset:2048 sc1\n\t"
    "buffer_store_dwordx4 v[60:63], %[lane16], %[srd], s87 offen offset:3072 sc1\n\t"
    "L_left3_%=:\n\t"
    "s_nop 7\n\t"
    "v_cvt_pk_bf16_f32 v64, v136, v137\n\t"
    "v_cvt_pk_bf16_f32 v65, v138, v139\n\t"
    "v_cvt_pk_bf16_f32 v66, v156, v157\n\t"
    "v_cvt_pk_bf16_f32 v67, v158, v159\n\t"
    "v_cvt_pk_bf16_f32 v68, v176, v177\n\t"
    "v_cvt_pk_bf16_f32 v69, v178, v179\n\t"
    "v_cvt_pk_bf16_f32 v70, v196, v197\n\t"
    "v_cvt_pk_bf16_f32 v71, v198, v199\n\t"
    "v_cvt_pk_bf16_f32 v72, v216, v217\n\t"
    "v_cvt_pk_bf16_f32 v73, v218, v219\n\t"
    "v_accvgpr_read_b32 v223, a160\n\t"
    "v_accvgpr_read_b32 v224, a161\n\t"
    "v_accvgpr_read_b32 v225, a162\n\t"
    "v_accvgpr_read_b32 v226, a163\n\t"
    "v_cvt_pk_bf16_f32 v74, v223, v224\n\t"
    "v_cvt_pk_bf16_f32 v75, v225, v226\n\t"
    "v_accvgpr_read_b32 v223, a180\n\t"
    "v_accvgpr_read_b32 v224, a181\n\t"
    "v_accvgpr_read_b32 v225, a182\n\t"
    "v_accvgpr_read_b32 v226, a183\n\t"
    "v_cvt_pk_bf16_f32 v76, v223, v224\n\t"
    "v_cvt_pk_bf16_f32 v77, v225, v226\n\t"
    "v_accvgpr_read_b32 v223, a200\n\t"
    "v_accvgpr_read_b32 v224, a201\n\t"
    "v_accvgpr_read_b32 v225, a202\n\t"
    "v_accvgpr_read_b32 v226, a203\n\t"
    "v_cvt_pk_bf16_f32 v78, v223, v224\n\t"
    "v_cvt_pk_bf16_f32 v79, v225, v226\n\t"
    "s_cmp_eq_u32 %[own4], 0\n\t"
    "s_cbranch_scc1 L_glob4_%=\n\t"
    "v_add_u32_e32 v223, %[own4], %[lane16]\n\t"
    "ds_write_b128 v223, v[64:67] offset:0\n\t"
    "ds_write_b128 v223, v[68:71] offset:1024\n\t"
    "ds_write_b128 v223, v[72:75] offset:2048\n\t"
    "ds_write_b128 v223, v[76:79] offset:3072\n\t"
    "s_branch L_left4_%=\n\t"
    "L_glob4_%=:\n\t"
    "buffer_store_dwordx4 v[64:67], %[lane16], %[srd], s88 offen sc1\n\t"
    "buffer_store_dwordx4 v[68:71], %[lane16], %[srd], s88 offen offset:1024 sc1\n\t"
    "buffer_store_dwordx4 v[72:75], %[lane16], %[srd], s88 offen offset:2048 sc1\n\t"
    "buffer_store_dwordx4 v[76:79], %[lane16], %[srd], s88 offen offset:3072 sc1\n\t"
    "L_left4_%=:\n\t"
    "s_waitcnt lgkmcnt(0)\n\t"
    "s_branch L_attn_end_%=\n\t"
    "L_attn_slow_%=:\n\t"
    "s_waitcnt vmcnt(0)\n\t"
    "s_barrier\n\t"
    "s_mov_b32 %[slow], 1\n\t"
    "L_attn_end_%=:\n\t"
      : [slow] "=&s"(slow)
      : [foff] "s"(foff), [flane] "v"(flane), [srd] "s"(srd), [hsrd] "s"(hsrd), [hoff] "s"(hoff), [woff] "s"(woff), [qoff] "s"(qoff), [pooff] "s"(pooff), [wlds] "s"(wlds), [stoff] "s"(stoff),
        [kb] "v"(kb), [vb0] "v"(vb0), [vb1] "v"(vb1), [lane16] "v"(lane16),
        [dv0] "v"(dv0), [dv1] "v"(dv1), [dv2] "v"(dv2), [dv3] "v"(dv3), [wlane] "v"(wlane), [pc2] "s"(pc2), [coff] "v"(coff),
        [own0] "s"(own[0]), [own1] "s"(own[1]), [own2] "s"(own[2]), [own3] "s"(own[3]), [own4] "s"(own[4])
      : "v0", "v1", "v2", "v3", "v4", "v5", "v6", "v7", "v8", "v9", "v10", "v11", "v12", "v13", "v14", "v15", "v16", "v17", "v18", "v19", "v20", "v21", "v22", "v23", "v24", "v25", "v26", "v27", "v28", "v29", "v30", "v31", "v32", "v33", "v34", "v35", "v36", "v37", "v38", "v39", "v40", "v41", "v42", "v43", "v44", "v45", "v46", "v47", "v48", "v49", "v50", "v51", "v52", "v53", "v54", "v55", "v56", "v57", "v58", "v59", "v60", "v61", "v62", "v63", "v64", "v65", "v66", "v67", "v68", "v69", "v70", "v71", "v72", "v73", "v74", "v75", "v76", "v77", "v78", "v79", "v80", "v81", "v82", "v83", "v84", "v85", "v86", "v87", "v88", "v89", "v90", "v91", "v92", "v93", "v94", "v95", "v96", "v97", "v98", "v99", "v100", "v101", "v102", "v103", "v104", "v105", "v106", "v107", "v108", "v109", "v110", "v111", "v112", "v113", "v114", "v115", "v116", "v117", "v118", "v119", "v120", "v121", "v122", "v123", "v124", "v125", "v126", "v127", "v128", "v129", "v130", "v131", "v132", "v133", "v134", "v135", "v136", "v137", "v138", "v139", "v140", "v141", "v142", "v143", "v144", "v145", "v146", "v147", "v148", "v149", "v150", "v151", "v152", "v153", "v154", "v155", "v156", "v157", "v158", "v159", "v160", "v161", "v162", "v163", "v164", "v165", "v166", "v167", "v168", "v169", "v170", "v171", "v172", "v173", "v174", "v175", "v176", "v177", "v178", "v179", "v180", "v181", "v182", "v183", "v184", "v185", "v186", "v187", "v188", "v189", "v190", "v191", "v192", "v193", "v194", "v195", "v196", "v197", "v198", "v199", "v200", "v201", "v202", "v203", "v204", "v205", "v206", "v207", "v208", "v209", "v210", "v211", "v212", "v213", "v214", "v215", "v216", "v217", "v218", "v219", "v220", "v221", "v222", "v223", "v224", "v225", "v226", "a0", "a1", "a2", "a3", "a4", "a5", "a6", "a7", "a8", "a9", "a10", "a11", "a12", "a13", "a14", "a15", "a16", "a17", "a18", "a19", "a20", "a21", "a22", "a23", "a24", "a25", "a26", "a27", "a28", "a29", "a30", "a31", "a32", "a33", "a34", "a35", "a36", "a37", "a38", "a39", "a40", "a41", "a42", "a43", "a44", "a45", "a46", "a47", "a48", "a49", "a50", "a51", "a52", "a53", "a54", "a55", "a56", "a57", "a58", "a59", "a60", "a61", "a62", "a63", "a64", "a65", "a66", "a67", "a68", "a69", "a70", "a71", "a72", "a73", "a74", "a75", "a76", "a77", "a78", "a79", "a80", "a81", "a82", "a83", "a84", "a85", "a86", "a87", "a88", "a89", "a90", "a91", "a92", "a93", "a94", "a95", "a96", "a97", "a98", "a99", "a100", "a101", "a102", "a103", "a104", "a105", "a106", "a107", "a108", "a109", "a110", "a111", "a112", "a113", "a114", "a115", "a116", "a117", "a118", "a119", "a120", "a121", "a122", "a123", "a124", "a125", "a126", "a127", "a128", "a129", "a130", "a131", "a132", "a133", "a134", "a135", "a136", "a137", "a138", "a139", "a140", "a141", "a142", "a143", "a144", "a145", "a146", "a147", "a148", "a149", "a150", "a151", "a152", "a153", "a154", "a155", "a156", "a157", "a158", "a159", "a160", "a161", "a162", "a163", "a164", "a165", "a166", "a167", "a168", "a169", "a170", "a171", "a172", "a173", "a174", "a175", "a176", "a177", "a178", "a179", "a180", "a181", "a182", "a183", "a184", "a185", "a186", "a187", "a188", "a189", "a190", "a191", "a192", "a193", "a194", "a195", "a196", "a197", "a198", "a199", "a200", "a201", "a202", "a203", "s60", "s61", "s62", "s63", "s64", "s65", "s66", "s67", "s68", "s69", "s70", "s71", "s72", "s73", "s74", "s75", "s76", "s77", "s78", "s79", "s80", "s81", "s82", "s83", "s84", "s85", "s86", "s87", "s88", "s89", "m0", "vcc", "scc", "memory");
    if (__builtin_expect(slow != 0u, 0)) attn_truemax(ws, lds, 4u * NQT * b + NQT * rfl(threadIdx.x >> 6), s, threadIdx.x & 63u, rfl(threadIdx.x >> 6));
    STAMP(ws, 4096u + bid * 4u + (threadIdx.x >> 6), 0);
    final_stage(x, ws, out, lds, b, s);
    STAMP(ws, 4096u + bid * 4u + (threadIdx.x >> 6), 6);
    CODE_MARK("L_gps_k2e");
  }
}

extern "C" void kernel_launch(void* const* d_in, const int* in_sizes, int n_in, void* d_out, int out_size, void* d_ws, size_t ws_size, hipStream_t stream) {
  const uint16_t* x = (const uint16_t*)d_in[0];
  const int* src = (const int*)d_in[1];
  const int* dst = (const int*)d_in[2];
  const uint16_t* deg_inv = (const uint16_t*)d_in[3];
  const uint16_t* w_msg = (const uint16_t*)d_in[4];
  const uint16_t* wq = (const uint16_t*)d_in[5];
  const uint16_t* wk = (const uint16_t*)d_in[6];
  const uint16_t* wv = (const uint16_t*)d_in[7];
  const uint16_t* w_out = (const uint16_t*)d_in[8];
  char* ws = (char*)d_ws;
    (void)hipFuncSetAttribute((const void*)k1_prep, hipFuncAttributeMaxDynamicSharedMemorySize, 65536);
  (void)hipFuncSetAttribute((const void*)k2_main, hipFuncAttributeMaxDynamicSharedMemorySize, K2LDS);
  hipLaunchKernelGGL(k1_prep, dim3(256), dim3(256), 65536, stream, x, src, dst, wq, wk, ws);
  hipLaunchKernelGGL(k2_main, dim3(256), dim3(256), K2LDS, stream, w_msg, wv, w_out, deg_inv, x, ws, (uint16_t*)d_out);
}
```
